```python
import torch

def _softmax(x, axis=-1):
    return torch.softmax(x, dim=axis)
def _gelu(x):
    return torch.nn.functional.gelu(x, approximate="tanh")
def _silu(x):
    return torch.nn.functional.silu(x)
def _sigmoid(x):
    return torch.sigmoid(x)
def _layernorm(x, axis=-1, eps=1e-5):
    m = x.mean(dim=axis, keepdim=True)
    v = x.var(dim=axis, keepdim=True, unbiased=False)
    return (x - m) / torch.sqrt(v + eps)
def _scatter_add(tgt, idx, src, axis=0):
    return tgt.clone().index_add_(axis, idx, src)
def _segment_sum(src, seg, n):
    out = torch.zeros((n,) + src.shape[1:], dtype=src.dtype)
    return out.index_add_(0, seg, src)
def _pad(x, pw):
    flat = tuple(v for pair in reversed(pw) for v in pair)
    return torch.nn.functional.pad(x, flat)

kernel_name = 'parallel_sum__sliding_block_cached__swiglu_ffn__decode'

def setup_inputs():

    _w = (lambda a: (lambda t: t.to(torch.bfloat16) if t.is_floating_point() else t)(torch.as_tensor(a)))
    def _A_setup():
        B, Sq, D = 2, 1, 512
        Hq, Hkv, Sk, Dh = 32, 8, 32768, 128
        assert Hq % Hkv == 0, (Hq, Hkv)
        return {
            "x": _w((lambda *s: torch.randn(*s, dtype=torch.bfloat16) * 0.05)(B, Sq, D)),
            "k_cache": _w((lambda *s: torch.randn(*s, dtype=torch.bfloat16) * 3.2)(B, Hkv, Sk, Dh)),
            "v_cache": _w((lambda *s: torch.randn(*s, dtype=torch.bfloat16) * 1.6)(B, Hkv, Sk, Dh)),
            "wq": _w((lambda *s: torch.randn(*s, dtype=torch.bfloat16) * 3.2)(D, Hq * Dh) * (D ** -0.5)),
            "wo": _w((lambda *s: torch.randn(*s, dtype=torch.bfloat16) * 1.6)(Hq * Dh, D) * ((Hq * Dh) ** -0.5)),
        }
    _A_in = _A_setup()

    def _B_setup():
        B, S, D, F = 2, 1, 512, 1792
        return {
            "x": _w((lambda *s: torch.randn(*s, dtype=torch.bfloat16) * 0.05)(B, S, D)),
            "w_gate": _w((lambda *s: torch.randn(*s, dtype=torch.bfloat16) * 0.05)(D, F)),
            "w_up": _w((lambda *s: torch.randn(*s, dtype=torch.bfloat16) * 0.05)(D, F)),
            "w_down": _w((lambda *s: torch.randn(*s, dtype=torch.bfloat16) * 0.05)(F, D)),
        }
    _B_in = _B_setup()

    return {
        "A_x": _A_in["x"],
        "A_k_cache": _A_in["k_cache"],
        "A_v_cache": _A_in["v_cache"],
        "A_wq": _A_in["wq"],
        "A_wo": _A_in["wo"],
        "B_x": _B_in["x"],
        "B_w_gate": _B_in["w_gate"],
        "B_w_up": _B_in["w_up"],
        "B_w_down": _B_in["w_down"],
    }

def reference(A_x, A_k_cache, A_v_cache, A_wq, A_wo, B_x, B_w_gate, B_w_up, B_w_down):
    def _A_ref(x, k_cache, v_cache, wq, wo):
        B, Sq, D = x.shape
        Hq, Dh = 32, 128
        Hkv, Sk = k_cache.shape[1], k_cache.shape[2]
        W = min(4096, max(256, Sk // 4))
        rep = Hq // Hkv
        q = (lambda x, *d: x.permute(*d))((x @ wq).reshape(B, Sq, Hq, Dh), 0, 2, 1, 3)
        k = (lambda x, n, axis: torch.repeat_interleave(x, n, dim=axis))(k_cache[:, :, Sk - W:, :], rep, axis=1)
        v = (lambda x, n, axis: torch.repeat_interleave(x, n, dim=axis))(v_cache[:, :, Sk - W:, :], rep, axis=1)
        scores = torch.einsum("bhqd,bhkd->bhqk", q, k) * (Dh ** -0.5)
        p = _softmax(scores, axis=-1)
        o = torch.einsum("bhqk,bhkd->bhqd", p, v)
        return (lambda x, *d: x.permute(*d))(o, 0, 2, 1, 3).reshape(B, Sq, Hq * Dh) @ wo

    def _B_ref(x, w_gate, w_up, w_down):
        g = _silu(x @ w_gate)
        u = x @ w_up
        return (g * u) @ w_down

    x = A_x
    xn = _layernorm(x, axis=-1)
    return x + _A_ref(xn, A_k_cache, A_v_cache, A_wq, A_wo) + _B_ref(xn, B_w_gate, B_w_up, B_w_down)
```

```cpp
#include <hip/hip_runtime.h>
#include <hip/hip_bf16.h>
#include <stdint.h>

typedef unsigned v4u __attribute__((ext_vector_type(4)));
typedef unsigned v2u __attribute__((ext_vector_type(2)));
typedef float f32x4 __attribute__((ext_vector_type(4)));
typedef float f32x2 __attribute__((ext_vector_type(2)));
typedef short bf16x8 __attribute__((ext_vector_type(8)));
typedef __bf16 bf16x2 __attribute__((ext_vector_type(2)));
typedef unsigned long long u64;

constexpr int DM = 512, HQ = 32, HKV = 8, SK = 32768, DH = 128, WIN = 4096, FF = 1792;
constexpr int NSPLIT = 16, KPW = WIN / NSPLIT;
constexpr int NWG = 256, NTHR = 512;
constexpr unsigned SPIN_CAP = 1u << 18;

constexpr int WS_Q = 4096;
constexpr int WS_H = WS_Q + 2 * 1024 * 16;
constexpr int WS_ML = WS_H + 16384;
constexpr int WS_PO = WS_ML + 16384;
constexpr int WS_FA = WS_PO + 1048576;
constexpr int WS_FB = WS_FA + 1048576;
constexpr int WS_END = WS_FB + 1048576;

struct Params {
  const unsigned short* x;
  const unsigned short* kc;
  const unsigned short* vc;
  const unsigned short* wq;
  const unsigned short* wo;
  const unsigned short* wg;
  const unsigned short* wu;
  const unsigned short* wd;
  unsigned short* out;
  unsigned char* ws;
};

__device__ __forceinline__ float bflo(unsigned u) { return __builtin_bit_cast(float, u << 16); }
__device__ __forceinline__ float bfhi(unsigned u) { return __builtin_bit_cast(float, u & 0xffff0000u); }
__device__ __forceinline__ unsigned cvtpk(float lo, float hi) {
  unsigned r;
  asm("v_cvt_pk_bf16_f32 %0, %1, %2" : "=v"(r) : "v"(lo), "v"(hi));
  return r;
}
__device__ __forceinline__ float bfr(float x) { return bflo(cvtpk(x, x)); }
__device__ __forceinline__ float dot2(unsigned a, unsigned b, float acc) {
  return __builtin_amdgcn_fdot2_f32_bf16(__builtin_bit_cast(bf16x2, a), __builtin_bit_cast(bf16x2, b), acc, false);
}
template <int CTRL> __device__ __forceinline__ float dpp(float x) {
  return __builtin_bit_cast(float, __builtin_amdgcn_mov_dpp(__builtin_bit_cast(int, x), CTRL, 0xf, 0xf, true));
}
constexpr int XOR1 = 0xB1, XOR2 = 0x4E, XOR7 = 0x141, MIRR = 0x140, XOR8 = 0x128, ROR4 = 0x124;
__device__ __forceinline__ float tr32(float a, float b) {
  auto s = __builtin_amdgcn_permlane32_swap(__float_as_uint(a), __float_as_uint(b), false, false);
  return __uint_as_float(s[0]) + __uint_as_float(s[1]);
}
__device__ __forceinline__ float tr16(float a, float b) {
  auto s = __builtin_amdgcn_permlane16_swap(__float_as_uint(a), __float_as_uint(b), false, false);
  return __uint_as_float(s[0]) + __uint_as_float(s[1]);
}
__device__ __forceinline__ float row16_sum(float x) {
  x += dpp<XOR1>(x); x += dpp<XOR2>(x); x += dpp<XOR7>(x); x += dpp<MIRR>(x);
  return x;
}
__device__ __forceinline__ float row16_max(float x) {
  x = fmaxf(x, dpp<XOR1>(x)); x = fmaxf(x, dpp<XOR2>(x)); x = fmaxf(x, dpp<XOR7>(x)); x = fmaxf(x, dpp<MIRR>(x));
  return x;
}
__device__ __forceinline__ float wave_sum(float x) {
  x = row16_sum(x);
  x = tr16(x, x);
  x = tr32(x, x);
  return x;
}
__device__ __forceinline__ v4u ldnt(const unsigned short* p) { return __builtin_nontemporal_load(reinterpret_cast<const v4u*>(p)); }
__device__ __forceinline__ v4u ldpl(const unsigned short* p) { return *reinterpret_cast<const v4u*>(p); }
__device__ __forceinline__ v4u ld_sc1(__amdgpu_buffer_rsrc_t rs, int off) { return __builtin_amdgcn_raw_buffer_load_b128(rs, off, 0, 16); }
__device__ __forceinline__ void st_sc1(__amdgpu_buffer_rsrc_t rs, int off, v4u v) { __builtin_amdgcn_raw_buffer_store_b128(v, rs, off, 0, 16); }
__device__ __forceinline__ f32x4 mfma(v4u a, v4u b, f32x4 c) {
  return __builtin_amdgcn_mfma_f32_16x16x32_bf16(__builtin_bit_cast(bf16x8, a), __builtin_bit_cast(bf16x8, b), c, 0, 0, 0);
}
#define SCHED_FENCE() __builtin_amdgcn_sched_barrier(0)
#define MEM_FENCE() asm volatile("" ::: "memory")

constexpr float LOG2E = 1.4426950408889634f;
constexpr float SCALE = 0.08838834764831845f;

constexpr int L_PR = 0;
constexpr int L_Q = L_PR + 512;
constexpr int L_P = L_Q + 2048;
constexpr int L_MG = L_P + 1024;
constexpr int L_ML = L_MG + 4096;
constexpr int L_CB = L_ML + 64;
constexpr int L_O = L_CB + 256;
constexpr int L_R3 = L_O + 32;
constexpr int L_END = L_R3 + 8192;

__global__ void __launch_bounds__(NTHR, 2) fused_decode_kernel(Params p) {
  __shared__ __attribute__((aligned(16))) float lds[L_END];
  const int tid = threadIdx.x, lane = tid & 63, wave = __builtin_amdgcn_readfirstlane(tid >> 6);
  const int bid = blockIdx.x, xcd = bid & 7, idx = bid >> 3;
  const __amdgpu_buffer_rsrc_t rs = __builtin_amdgcn_make_buffer_rsrc(p.ws, 0, WS_END, 0x00020000);

  const u64 epoch = *reinterpret_cast<const __attribute__((address_space(4))) u64*>(reinterpret_cast<uintptr_t>(p.ws));
  const v4u xa = ldpl(p.x + 8 * lane), xb = ldpl(p.x + DM + 8 * lane);
  const int kind = wave >> 2;
  const int t2 = tid & 255, half = t2 & 1, rq = t2 >> 1;
  const v2u xr0 = *reinterpret_cast<const v2u*>(p.x + 4 * rq), xr1 = *reinterpret_cast<const v2u*>(p.x + DM + 4 * rq);
  const bool has_gu = idx < 28;
  v4u w[4];
  {
    const unsigned short* wp;
    int stride;
    if (kind == 0) { wp = p.wq + (xcd * 512 + idx * 16 + half * 8); stride = HQ * DH; }
    else { wp = (half ? p.wu : p.wg) + (xcd * 28 + (has_gu ? idx : 0)) * 8; stride = FF; }
    wp += (size_t)(4 * rq) * stride;
#pragma unroll
    for (int j = 0; j < 4; ++j) w[j] = ldpl(wp + (size_t)j * stride);
  }
  SCHED_FENCE();
  const int ab = idx >> 4, asplit = idx & 15, kvh = xcd;
  const size_t kvbase = ((size_t)(ab * HKV + kvh) * SK + (SK - WIN) + asplit * KPW + wave * 32) * DH;
  v4u kf[2][4];
  {
    const unsigned short* kp = p.kc + kvbase + (lane & 15) * DH + (lane >> 4) * 8;
#pragma unroll
    for (int T = 0; T < 2; ++T)
#pragma unroll
      for (int s = 0; s < 4; ++s) kf[T][s] = ldnt(kp + T * 16 * DH + s * 32);
  }
  SCHED_FENCE();

  const u64 tag64 = (epoch + 1ull) * 0x9E3779B97F4A7C15ull;
  const unsigned tA = (unsigned)tag64, tB = (unsigned)(tag64 >> 32);

  float mean0, mean1, den0, den1;
  {
    float e0[8], e1[8];
#pragma unroll
    for (int k = 0; k < 4; ++k) { e0[2 * k] = bflo(xa[k]); e0[2 * k + 1] = bfhi(xa[k]); e1[2 * k] = bflo(xb[k]); e1[2 * k + 1] = bfhi(xb[k]); }
    float s0 = 0.f, s1 = 0.f;
#pragma unroll
    for (int k = 0; k < 8; ++k) { s0 += e0[k]; s1 += e1[k]; }
    s0 = wave_sum(s0) * (1.f / DM); s1 = wave_sum(s1) * (1.f / DM);
    float q0 = 0.f, q1 = 0.f;
#pragma unroll
    for (int k = 0; k < 8; ++k) { const float d0 = e0[k] - s0, d1 = e1[k] - s1; q0 = fmaf(d0, d0, q0); q1 = fmaf(d1, d1, q1); }
    q0 = wave_sum(q0) * (1.f / DM); q1 = wave_sum(q1) * (1.f / DM);
    mean0 = bfr(s0); mean1 = bfr(s1);
    den0 = bfr(sqrtf(bfr(bfr(q0) + 1e-5f))); den1 = bfr(sqrtf(bfr(bfr(q1) + 1e-5f)));
  }
  unsigned xn0[2], xn1[2];
#pragma unroll
  for (int k = 0; k < 2; ++k) {
    xn0[k] = cvtpk(bfr(bflo(xr0[k]) - mean0) / den0, bfr(bfhi(xr0[k]) - mean0) / den0);
    xn1[k] = cvtpk(bfr(bflo(xr1[k]) - mean1) / den1, bfr(bfhi(xr1[k]) - mean1) / den1);
  }
  {
    float a[16];
#pragma unroll
    for (int k = 0; k < 16; ++k) a[k] = 0.f;
#pragma unroll
    for (int pr = 0; pr < 2; ++pr)
#pragma unroll
      for (int k = 0; k < 4; ++k) {
        const unsigned lo = __builtin_amdgcn_perm(w[2 * pr + 1][k], w[2 * pr][k], 0x05040100u);
        const unsigned hi = __builtin_amdgcn_perm(w[2 * pr + 1][k], w[2 * pr][k], 0x07060302u);
        a[2 * k] = dot2(lo, xn0[pr], a[2 * k]); a[2 * k + 1] = dot2(hi, xn0[pr], a[2 * k + 1]);
        a[8 + 2 * k] = dot2(lo, xn1[pr], a[8 + 2 * k]); a[8 + 2 * k + 1] = dot2(hi, xn1[pr], a[8 + 2 * k + 1]);
      }
#pragma unroll
    for (int k = 0; k < 8; ++k) a[k] = tr32(a[k], a[k + 8]);
#pragma unroll
    for (int k = 0; k < 4; ++k) a[k] = tr16(a[k], a[k + 4]);
#pragma unroll
    for (int k = 0; k < 2; ++k) { const float u0 = a[k] + dpp<ROR4>(a[k]), u1 = a[k + 2] + dpp<ROR4>(a[k + 2]); a[k] = (lane & 4) ? u1 : u0; }
    { const float u0 = a[0] + dpp<XOR8>(a[0]), u1 = a[1] + dpp<XOR8>(a[1]); a[0] = (lane & 8) ? u1 : u0; }
    a[0] += dpp<XOR2>(a[0]);
    lds[L_PR + wave * 64 + lane] = a[0];
  }
  asm volatile("" ::"s"(tA), "s"(tB));
  __syncthreads();
#define PL(b, c8, h) (((b) << 5) | ((((c8) >> 2) & 1) << 4) | ((((c8) >> 1) & 1) << 2) | (((c8) & 1) << 3) | (h))
  if (wave == 0 && lane < 8) {
    const int b = lane >> 2, cq = lane & 3, h = cq >> 1, c0 = (cq & 1) * 4;
    float v[4];
#pragma unroll
    for (int k = 0; k < 4; ++k) {
      const int l = PL(b, c0 + k, h);
      v[k] = (lds[L_PR + l] + lds[L_PR + 64 + l]) + (lds[L_PR + 128 + l] + lds[L_PR + 192 + l]);
    }
    const v4u g = {tA, cvtpk(v[0], v[1]), tB, cvtpk(v[2], v[3])};
    st_sc1(rs, WS_Q + (b * 1024 + xcd * 128 + idx * 4 + cq) * 16, g);
  }
  if (wave == 4 && lane < 4 && has_gu) {
    float hv[4];
#pragma unroll
    for (int k = 0; k < 4; ++k) {
      const int b = k & 1, c8 = 2 * lane + (k >> 1);
      const int lg = PL(b, c8, 0), lu = PL(b, c8, 1);
      const float g = bfr((lds[L_PR + 256 + lg] + lds[L_PR + 320 + lg]) + (lds[L_PR + 384 + lg] + lds[L_PR + 448 + lg]));
      const float u = bfr((lds[L_PR + 256 + lu] + lds[L_PR + 320 + lu]) + (lds[L_PR + 384 + lu] + lds[L_PR + 448 + lu]));
      const float sg = bfr(g / (1.f + __expf(-g)));
      hv[k] = sg * u;
    }
    const v4u g = {tA, cvtpk(hv[0], hv[1]), tB, cvtpk(hv[2], hv[3])};
    st_sc1(rs, WS_H + ((xcd * 28 + idx) * 4 + lane) * 16, g);
  }
#undef PL

  const int qoff = WS_Q + (ab * 1024 + kvh * 128 + lane) * 16;
  v4u g0 = ld_sc1(rs, qoff), g1 = ld_sc1(rs, qoff + 1024);
  SCHED_FENCE();
  v4u vf[8];
  {
    const unsigned short* vp = p.vc + kvbase + (lane >> 4) * DH + (lane & 15) * 8;
#pragma unroll
    for (int j = 0; j < 8; ++j) vf[j] = ldnt(vp + j * 4 * DH);
  }
  const int rgi = xcd * 16 + (idx >> 1), ch = idx & 1;
  const int cc = tid & 31, rg = tid >> 5;
  v4u wo0, wo1, wdv;
  {
    const unsigned short* op = p.wo + (size_t)(32 * rgi + 2 * rg) * DM + 256 * ch + 8 * cc;
    wo0 = ldnt(op); wo1 = ldnt(op + DM);
    wdv = ldnt(p.wd + (size_t)(14 * rgi + (rg < 14 ? rg : 13)) * DM + 256 * ch + 8 * cc);
  }
  SCHED_FENCE();
  if (!__all(g0.x == tA && g0.z == tB && g1.x == tA && g1.z == tB)) {
    for (unsigned spins = 0; spins < SPIN_CAP; ++spins) {
      MEM_FENCE();
      g0 = ld_sc1(rs, qoff); g1 = ld_sc1(rs, qoff + 1024);
      if (__all(g0.x == tA && g0.z == tB && g1.x == tA && g1.z == tB)) break;
    }
  }
  v4u qf[4];
  {
    unsigned* qs = reinterpret_cast<unsigned*>(lds + L_Q + wave * 256);
    *reinterpret_cast<v2u*>(qs + 2 * lane) = v2u{g0.y, g0.w};
    *reinterpret_cast<v2u*>(qs + 128 + 2 * lane) = v2u{g1.y, g1.w};
    const int h = lane & 15;
#pragma unroll
    for (int s = 0; s < 4; ++s) {
      v4u t = *reinterpret_cast<const v4u*>(qs + (h & 3) * 64 + s * 16 + (lane >> 4) * 4);
      qf[s] = h < 4 ? t : v4u{0u, 0u, 0u, 0u};
    }
  }
  float mw[4], lw[4];
  {
    f32x4 sc[2];
#pragma unroll
    for (int T = 0; T < 2; ++T) {
      sc[T] = f32x4{0.f, 0.f, 0.f, 0.f};
#pragma unroll
      for (int s = 0; s < 4; ++s) sc[T] = mfma(qf[s], kf[T][s], sc[T]);
    }
    float* ps = lds + L_P + wave * 128;
#pragma unroll
    for (int h = 0; h < 4; ++h) {
      const float s0 = bfr(bfr(sc[0][h]) * SCALE), s1 = bfr(bfr(sc[1][h]) * SCALE);
      const float m = row16_max(fmaxf(s0, s1));
      const float p0 = __builtin_amdgcn_exp2f((s0 - m) * LOG2E), p1 = __builtin_amdgcn_exp2f((s1 - m) * LOG2E);
      mw[h] = m; lw[h] = row16_sum(p0 + p1);
      sc[0][h] = p0; sc[1][h] = p1;
    }
    if (lane < 16) {
      *reinterpret_cast<f32x4*>(ps + lane * 4) = sc[0];
      *reinterpret_cast<f32x4*>(ps + 64 + lane * 4) = sc[1];
    }
  }
  {
    const float* ps = lds + L_P + wave * 128;
    float acc[4][8];
#pragma unroll
    for (int h = 0; h < 4; ++h)
#pragma unroll
      for (int k = 0; k < 8; ++k) acc[h][k] = 0.f;
#pragma unroll
    for (int j = 0; j < 8; ++j) {
      const f32x4 pv = *reinterpret_cast<const f32x4*>(ps + ((lane >> 4) + 4 * j) * 4);
      float e[8];
#pragma unroll
      for (int k = 0; k < 4; ++k) { e[2 * k] = bflo(vf[j][k]); e[2 * k + 1] = bfhi(vf[j][k]); }
#pragma unroll
      for (int h = 0; h < 4; ++h)
#pragma unroll
        for (int k = 0; k < 8; ++k) acc[h][k] = fmaf(pv[h], e[k], acc[h][k]);
    }
    float r[8];
#pragma unroll
    for (int k = 0; k < 8; ++k) {
      const float u0 = tr32(acc[0][k], acc[2][k]), u1 = tr32(acc[1][k], acc[3][k]);
      r[k] = tr16(u0, u1);
    }
    float* mg = lds + L_MG + wave * 512 + (lane >> 4) * 128 + (lane & 15) * 8;
    *reinterpret_cast<f32x4*>(mg) = f32x4{r[0], r[1], r[2], r[3]};
    *reinterpret_cast<f32x4*>(mg + 4) = f32x4{r[4], r[5], r[6], r[7]};
    if (lane == 0) {
#pragma unroll
      for (int h = 0; h < 4; ++h) { lds[L_ML + wave * 8 + 2 * h] = mw[h]; lds[L_ML + wave * 8 + 2 * h + 1] = lw[h]; }
    }
  }
  __syncthreads();
  if (tid < 256) {
    const int h = tid >> 6, dp = tid & 63;
    float M = lds[L_ML + 2 * h];
#pragma unroll
    for (int w8 = 1; w8 < 8; ++w8) M = fmaxf(M, lds[L_ML + w8 * 8 + 2 * h]);
    float o0 = 0.f, o1 = 0.f, L = 0.f;
#pragma unroll
    for (int w8 = 0; w8 < 8; ++w8) {
      const float wt = __builtin_amdgcn_exp2f((lds[L_ML + w8 * 8 + 2 * h] - M) * LOG2E);
      const f32x2 a = *reinterpret_cast<const f32x2*>(lds + L_MG + w8 * 512 + h * 128 + 2 * dp);
      o0 = fmaf(wt, a.x, o0); o1 = fmaf(wt, a.y, o1); L = fmaf(wt, lds[L_ML + w8 * 8 + 2 * h + 1], L);
    }
    const int rec = (ab * HQ + kvh * 4 + h) * NSPLIT + asplit;
    st_sc1(rs, WS_PO + (rec * 64 + dp) * 16, v4u{tA, __float_as_uint(o0), tB, __float_as_uint(o1)});
    if (dp == 0) st_sc1(rs, WS_ML + rec * 16, v4u{tA, __float_as_uint(M), tB, __float_as_uint(L)});
  }

  unsigned hpk;
  {
    const int hoff = WS_H + (7 * rgi + ((rg < 14 ? rg : 13) >> 1)) * 16;
    v4u g = ld_sc1(rs, hoff);
    for (unsigned spins = 0; spins < SPIN_CAP; ++spins) {
      if (__all(g.x == tA && g.z == tB)) break;
      MEM_FENCE();
      g = ld_sc1(rs, hoff);
    }
    hpk = (rg & 1) ? g.w : g.y;
  }
  {
    const int cb = tid >> 8, cs = (tid >> 4) & 15, cdp = tid & 15, head = rgi >> 2;
    const int rec0 = (cb * HQ + head) * NSPLIT;
    const int ooff = WS_PO + ((rec0 + cs) * 64 + (rgi & 3) * 16 + cdp) * 16;
    const int moff = WS_ML + (rec0 + (lane & 15)) * 16;
    v4u go = ld_sc1(rs, ooff), gm = ld_sc1(rs, moff);
    for (unsigned spins = 0; spins < SPIN_CAP; ++spins) {
      if (__all(go.x == tA && go.z == tB && gm.x == tA && gm.z == tB)) break;
      MEM_FENCE();
      go = ld_sc1(rs, ooff); gm = ld_sc1(rs, moff);
    }
    const float ms = __uint_as_float(gm.y), ls = __uint_as_float(gm.w);
    const float M = row16_max(ms);
    const float wt = __builtin_amdgcn_exp2f((ms - M) * LOG2E);
    const float L = row16_sum(wt * ls);
    const float wn = wt / L;
    const float wsel = __int_as_float(__builtin_amdgcn_ds_bpermute(cs * 4, __float_as_int(wn)));
    float o0 = wsel * __uint_as_float(go.y), o1 = wsel * __uint_as_float(go.w);
    float r = tr32(o0, o1);
    r = tr16(r, r);
    if ((lane & 16) == 0) lds[L_CB + wave * 32 + (lane & 15) * 2 + (lane >> 5)] = r;
  }
  __syncthreads();
  if (tid < 32) {
    const int b = tid >> 4, dp = tid & 15;
    const float* c = lds + L_CB + b * 128 + dp * 2;
    const float o0 = (c[0] + c[32]) + (c[64] + c[96]), o1 = (c[1] + c[33]) + (c[65] + c[97]);
    reinterpret_cast<unsigned*>(lds)[L_O + tid] = cvtpk(o0, o1);
  }
  __syncthreads();
  {
    const unsigned ob0 = reinterpret_cast<const unsigned*>(lds)[L_O + rg], ob1 = reinterpret_cast<const unsigned*>(lds)[L_O + 16 + rg];
    float a[16], f[16];
    const float h0 = rg < 14 ? bflo(hpk) : 0.f, h1 = rg < 14 ? bfhi(hpk) : 0.f;
#pragma unroll
    for (int k = 0; k < 4; ++k) {
      const unsigned lo = __builtin_amdgcn_perm(wo1[k], wo0[k], 0x05040100u);
      const unsigned hi = __builtin_amdgcn_perm(wo1[k], wo0[k], 0x07060302u);
      a[2 * k] = dot2(lo, ob0, 0.f); a[2 * k + 1] = dot2(hi, ob0, 0.f);
      a[8 + 2 * k] = dot2(lo, ob1, 0.f); a[8 + 2 * k + 1] = dot2(hi, ob1, 0.f);
      const float d0 = bflo(wdv[k]), d1 = bfhi(wdv[k]);
      f[2 * k] = d0 * h0; f[2 * k + 1] = d1 * h0; f[8 + 2 * k] = d0 * h1; f[8 + 2 * k + 1] = d1 * h1;
    }
    float* r3 = lds + L_R3 + wave * 1024 + lane * 16;
    float ra[8], rf[8];
#pragma unroll
    for (int k = 0; k < 8; ++k) { ra[k] = tr32(a[k], a[k + 8]); rf[k] = tr32(f[k], f[k + 8]); }
    *reinterpret_cast<f32x4*>(r3) = f32x4{ra[0], ra[1], ra[2], ra[3]};
    *reinterpret_cast<f32x4*>(r3 + 4) = f32x4{ra[4], ra[5], ra[6], ra[7]};
    *reinterpret_cast<f32x4*>(r3 + 8) = f32x4{rf[0], rf[1], rf[2], rf[3]};
    *reinterpret_cast<f32x4*>(r3 + 12) = f32x4{rf[4], rf[5], rf[6], rf[7]};
  }
  __syncthreads();
  {
    const int kd = tid >> 8, b = (tid >> 7) & 1, cp = tid & 127;
    const float* src = lds + L_R3 + (b * 32 + (cp >> 2)) * 16 + kd * 8 + (cp & 3) * 2;
    float s0 = 0.f, s1 = 0.f;
#pragma unroll
    for (int w8 = 0; w8 < 8; ++w8) { const f32x2 v = *reinterpret_cast<const f32x2*>(src + w8 * 1024); s0 += v.x; s1 += v.y; }
    st_sc1(rs, (kd ? WS_FB : WS_FA) + (rgi * 512 + b * 256 + ch * 128 + cp) * 16, v4u{tA, __float_as_uint(s0), tB, __float_as_uint(s1)});
  }

  if (wave == 0) {
    const int pb = (xcd * 32 + idx) * 2;
    v4u g[8];
    const int o0 = (lane * 512 + pb) * 16;
    for (unsigned spins = 0; spins < SPIN_CAP; ++spins) {
      bool ok = true;
#pragma unroll
      for (int k = 0; k < 8; ++k) {
        g[k] = ld_sc1(rs, ((k & 4) ? WS_FB : WS_FA) + o0 + ((k >> 1) & 1) * 64 * 512 * 16 + (k & 1) * 16);
        ok = ok && g[k].x == tA && g[k].z == tB;
      }
      if (__all(ok)) break;
      MEM_FENCE();
    }
    float v[8];
#pragma unroll
    for (int kd = 0; kd < 2; ++kd)
#pragma unroll
      for (int pr = 0; pr < 2; ++pr) {
        v[kd * 4 + pr * 2] = __uint_as_float(g[kd * 4 + pr].y) + __uint_as_float(g[kd * 4 + 2 + pr].y);
        v[kd * 4 + pr * 2 + 1] = __uint_as_float(g[kd * 4 + pr].w) + __uint_as_float(g[kd * 4 + 2 + pr].w);
      }
#pragma unroll
    for (int k = 0; k < 8; ++k) v[k] = wave_sum(v[k]);
    if (lane == 0) {
      const v2u xv = *reinterpret_cast<const v2u*>(p.x + pb * 2);
      const float x0 = bflo(xv.x), x1 = bfhi(xv.x), x2 = bflo(xv.y), x3 = bfhi(xv.y);
      const float r0 = bfr(x0 + bfr(v[0])) + bfr(v[4]), r1 = bfr(x1 + bfr(v[1])) + bfr(v[5]);
      const float r2 = bfr(x2 + bfr(v[2])) + bfr(v[6]), r3 = bfr(x3 + bfr(v[3])) + bfr(v[7]);
      *reinterpret_cast<v2u*>(p.out + pb * 2) = v2u{cvtpk(r0, r1), cvtpk(r2, r3)};
      if (bid == 0) *reinterpret_cast<volatile u64*>(p.ws) = epoch + 1ull;
    }
  }
}

extern "C" void kernel_launch(void* const* d_in, const int* in_sizes, int n_in, void* d_out, int out_size, void* d_ws,
                              size_t ws_size, hipStream_t stream) {
  Params p;
  p.x = (const unsigned short*)d_in[0];
  p.kc = (const unsigned short*)d_in[1];
  p.vc = (const unsigned short*)d_in[2];
  p.wq = (const unsigned short*)d_in[3];
  p.wo = (const unsigned short*)d_in[4];
  p.wg = (const unsigned short*)d_in[6];
  p.wu = (const unsigned short*)d_in[7];
  p.wd = (const unsigned short*)d_in[8];
  p.out = (unsigned short*)d_out;
  p.ws = (unsigned char*)d_ws;
  hipLaunchKernelGGL(fused_decode_kernel, dim3(NWG), dim3(NTHR), 0, stream, p);
}
```

```cpp
#include <hip/hip_runtime.h>
#include <hip/hip_bf16.h>
#include <stdint.h>

typedef unsigned v4u __attribute__((ext_vector_type(4)));
typedef unsigned v2u __attribute__((ext_vector_type(2)));
typedef float f32x4 __attribute__((ext_vector_type(4)));
typedef float f32x2 __attribute__((ext_vector_type(2)));
typedef short bf16x8 __attribute__((ext_vector_type(8)));
typedef short s16x4 __attribute__((ext_vector_type(4)));
typedef unsigned long long u64;
#define LAS __attribute__((address_space(3)))

constexpr int DM = 512, HQ = 32, HKV = 8, SK = 32768, DH = 128, WIN = 4096, FF = 1792;
constexpr int NSPLIT = 16, KPW = WIN / NSPLIT;
constexpr int NWG = 256, NTHR = 512;
constexpr unsigned SPIN_CAP = 1u << 18;
#define KEARLY 2

constexpr int WS_Q = 4096;
constexpr int WS_G = WS_Q + 2 * 4 * 2048 * 16;
constexpr int WS_ML = WS_G + 16 * 896 * 16;
constexpr int WS_PO = WS_ML + 32768;
constexpr int WS_F = WS_PO + 1048576;
constexpr int WS_END = WS_F + 1048576;

constexpr int LK = 0;
constexpr int LV = 65536;
constexpr int LGU = LV + 49152;
constexpr int LW = 131072;
constexpr int LD = 147456;
constexpr int LS = 155648;
constexpr int S_XN = LS;
constexpr int XN_ST = 1040;
constexpr int S_Q = S_XN + 2 * XN_ST;
constexpr int Q_ST = 272;
constexpr int S_P = S_Q + 4 * Q_ST;
constexpr int P_ST = 528;
constexpr int S_MX = S_P + 4 * P_ST;
constexpr int S_LS = S_MX + 64;
constexpr int S_O = S_LS + 64;
constexpr int O_ST = 144;
constexpr int S_H = S_O + 2 * O_ST;
constexpr int H_ST = 80;
constexpr int LDS_BYTES = S_H + 2 * H_ST;

struct Params {
  const unsigned short* x;
  const unsigned short* kc;
  const unsigned short* vc;
  const unsigned short* wq;
  const unsigned short* wo;
  const unsigned short* wg;
  const unsigned short* wu;
  const unsigned short* wd;
  unsigned short* out;
  unsigned char* ws;
};

__device__ __forceinline__ float bflo(unsigned u) { return __builtin_bit_cast(float, u << 16); }
__device__ __forceinline__ float bfhi(unsigned u) { return __builtin_bit_cast(float, u & 0xffff0000u); }
typedef __bf16 bf16x2 __attribute__((ext_vector_type(2)));
__device__ __forceinline__ unsigned cvtpk(float lo, float hi) {
  return __builtin_bit_cast(unsigned, __builtin_convertvector((f32x2){lo, hi}, bf16x2));
}
__device__ __forceinline__ float bfr(float x) { return bflo(cvtpk(x, x)); }
template <int CTRL> __device__ __forceinline__ float dpp(float x) {
  return __builtin_bit_cast(float, __builtin_amdgcn_mov_dpp(__builtin_bit_cast(int, x), CTRL, 0xf, 0xf, true));
}
constexpr int XOR1 = 0xB1, XOR2 = 0x4E, XOR7 = 0x141, MIRR = 0x140, XOR8 = 0x128;
__device__ __forceinline__ float tr32(float a, float b) {
  auto s = __builtin_amdgcn_permlane32_swap(__float_as_uint(a), __float_as_uint(b), false, false);
  return __uint_as_float(s[0]) + __uint_as_float(s[1]);
}
__device__ __forceinline__ float tr16(float a, float b) {
  auto s = __builtin_amdgcn_permlane16_swap(__float_as_uint(a), __float_as_uint(b), false, false);
  return __uint_as_float(s[0]) + __uint_as_float(s[1]);
}
__device__ __forceinline__ float tr32m(float a, float b) {
  auto s = __builtin_amdgcn_permlane32_swap(__float_as_uint(a), __float_as_uint(b), false, false);
  return fmaxf(__uint_as_float(s[0]), __uint_as_float(s[1]));
}
__device__ __forceinline__ float tr16m(float a, float b) {
  auto s = __builtin_amdgcn_permlane16_swap(__float_as_uint(a), __float_as_uint(b), false, false);
  return fmaxf(__uint_as_float(s[0]), __uint_as_float(s[1]));
}
__device__ __forceinline__ float row16_sum(float x) {
  x += dpp<XOR1>(x); x += dpp<XOR2>(x); x += dpp<XOR7>(x); x += dpp<MIRR>(x);
  return x;
}
__device__ __forceinline__ float row16_max(float x) {
  x = fmaxf(x, dpp<XOR1>(x)); x = fmaxf(x, dpp<XOR2>(x)); x = fmaxf(x, dpp<XOR7>(x)); x = fmaxf(x, dpp<MIRR>(x));
  return x;
}
__device__ __forceinline__ float wave_sum(float x) {
  x = row16_sum(x);
  x = tr16(x, x);
  x = tr32(x, x);
  return x;
}
__device__ __forceinline__ v4u ld_sc1(__amdgpu_buffer_rsrc_t rs, int off) { return __builtin_amdgcn_raw_buffer_load_b128(rs, off, 0, 16); }
__device__ __forceinline__ void st_sc1(__amdgpu_buffer_rsrc_t rs, int off, v4u v) { __builtin_amdgcn_raw_buffer_store_b128(v, rs, off, 0, 16); }
__device__ __forceinline__ f32x4 mfma(v4u a, v4u b, f32x4 c) {
  return __builtin_amdgcn_mfma_f32_16x16x32_bf16(__builtin_bit_cast(bf16x8, a), __builtin_bit_cast(bf16x8, b), c, 0, 0, 0);
}
__device__ __forceinline__ v4u lds128(const LAS char* p) { return *reinterpret_cast<const LAS v4u*>(p); }
__device__ __forceinline__ v4u ldtr(const LAS char* a0, const LAS char* a1) {
  const s16x4 lo = __builtin_amdgcn_ds_read_tr16_b64_v4i16((LAS s16x4*)a0);
  const s16x4 hi = __builtin_amdgcn_ds_read_tr16_b64_v4i16((LAS s16x4*)a1);
  const v2u l2 = __builtin_bit_cast(v2u, lo), h2 = __builtin_bit_cast(v2u, hi);
  return v4u{l2.x, l2.y, h2.x, h2.y};
}
__device__ __forceinline__ int off256(int row, int ch) { return 256 * row + 16 * (ch ^ (((row & 3) << 2) | ((row >> 2) & 3))); }
__device__ __forceinline__ int offk(int row, int ch) { return 256 * row + 16 * (ch ^ (row & 15)); }
__device__ __forceinline__ int off128(int row, int ch) { return 128 * row + 16 * (ch ^ ((row & 2) | ((row >> 1) & 4))); }
__device__ __forceinline__ void dma1(const void* g, unsigned lds_dst) {
  unsigned keep;
  asm volatile("s_mov_b32 %0, m0\n\ts_mov_b32 m0, %2\n\ts_nop 0\n\tglobal_load_lds_dwordx4 %1, off nt\n\ts_mov_b32 m0, %0"
               : "=&s"(keep) : "v"(g), "s"(lds_dst) : "memory");
}
__device__ __forceinline__ void dma4(const void* sbase, unsigned v0, unsigned v1, unsigned v2, unsigned v3, unsigned lds_dst) {
  unsigned keep;
  asm volatile("s_mov_b32 %0, m0\n\ts_mov_b32 m0, %6\n\ts_nop 0\n\t"
               "global_load_lds_dwordx4 %1, %5 nt\n\t"
               "global_load_lds_dwordx4 %2, %5 offset:1024 nt\n\t"
               "global_load_lds_dwordx4 %3, %5 offset:2048 nt\n\t"
               "global_load_lds_dwordx4 %4, %5 offset:3072 nt\n\t"
               "s_mov_b32 m0, %0"
               : "=&s"(keep) : "v"(v0), "v"(v1), "v"(v2), "v"(v3), "s"(sbase), "s"(lds_dst) : "memory");
}
#define STAMP(k) do {} while (0)
#define STAMP_DECL do {} while (0)
#define STAMP_FLUSH do {} while (0)
#define MEM_FENCE() asm volatile("" ::: "memory")
#define BAR() asm volatile("s_waitcnt lgkmcnt(0)\n\ts_barrier" ::: "memory")
#define VMWAIT(n) asm volatile("s_waitcnt vmcnt(" #n ")" ::: "memory")
#define TAGOK(g) ((g).x == tA && (g).z == tB)

constexpr float LOG2E = 1.4426950408889634f;
constexpr float SCALE = 0.08838834764831845f;

__device__ __forceinline__ void proj_tile(const LAS char* sm, int tile, int kbase, int c, int lane, __amdgpu_buffer_rsrc_t rs,
                                          int dst, int bstride, unsigned tA, unsigned tB) {
  const int g = lane >> 4, q = (lane & 15) >> 2, pp = lane & 3;
  const LAS char* ap = sm + S_XN + (lane & 1) * XN_ST + (kbase + 8 * g) * 2;
  f32x4 acc = {0.f, 0.f, 0.f, 0.f};
#pragma unroll
  for (int ks = 0; ks < 4; ++ks) {
    const int r0 = 32 * ks + 8 * g + q;
    const v4u b = ldtr(sm + tile + off128(r0, 2 * c + (pp >> 1)) + 8 * (pp & 1), sm + tile + off128(r0 + 4, 2 * c + (pp >> 1)) + 8 * (pp & 1));
    acc = mfma(lds128(ap + ks * 64), b, acc);
  }
  const float n0 = dpp<XOR1>(acc[0]), n1 = dpp<XOR1>(acc[1]);
  const bool odd = lane & 1;
  const v4u gr = {tA, __float_as_uint(odd ? n1 : acc[0]), tB, __float_as_uint(odd ? acc[1] : n0)};
  if (lane < 16) st_sc1(rs, dst + (odd ? bstride : 0) + (c * 8 + (lane >> 1)) * 16, gr);
}

__global__ void __launch_bounds__(NTHR, 2) fused_decode_kernel(Params p) {
  extern __shared__ __attribute__((aligned(16))) char smem_[];
  const LAS char* smc = (const LAS char*)smem_;
  LAS char* sm = (LAS char*)smem_;
  const unsigned lb = (unsigned)(size_t)sm;
  const int tid = threadIdx.x, lane = tid & 63, wave = __builtin_amdgcn_readfirstlane(tid >> 6);
  const int bid = blockIdx.x, xcd = bid & 7, idx = bid >> 3;
  const __amdgpu_buffer_rsrc_t rs = __builtin_amdgcn_make_buffer_rsrc(p.ws, 0, WS_END, 0x00020000);
  STAMP_DECL;
  STAMP(0);
  const u64 epoch = *reinterpret_cast<const __attribute__((address_space(4))) u64*>(reinterpret_cast<uintptr_t>(p.ws));
  const u64 tag64 = (epoch + 1ull) * 0x9E3779B97F4A7C15ull;
  const unsigned tA = (unsigned)tag64, tB = (unsigned)(tag64 >> 32);

  const int qks = idx >> 3, qcol = xcd * 512 + (idx & 7) * 64;
  const int gtile = bid < 224 ? bid : 0, gmat = gtile / 112, gct = (gtile % 112) >> 2, gks = gtile & 3;
  const int ab = idx >> 4, asplit = idx & 15, kvh = xcd;
  const int rgi = xcd * 8 + (idx >> 2), cq = idx & 3, hp = (rgi >> 2) & 1, dq = rgi & 3;
  const int g4 = lane >> 4, q4 = (lane & 15) >> 2, p4 = lane & 3;

  if (wave >= 4) {
    const int lw = wave - 4;
    {
      const int r = 32 * lw + (lane >> 3);
#pragma unroll
      for (int j = 0; j < 4; ++j) {
        const int row = r + 8 * j, ch = (lane & 7) ^ ((row & 2) | ((row >> 1) & 4));
        dma1(p.wq + (size_t)(qks * 128 + row) * (HQ * DH) + qcol + ch * 8, lb + LW + (32 * lw + 8 * j) * 128);
      }
      const unsigned short* wgu = gmat ? p.wu : p.wg;
      if (bid < 224) {
#pragma unroll
        for (int j = 0; j < 4; ++j) {
          const int row = r + 8 * j, ch = (lane & 7) ^ ((row & 2) | ((row >> 1) & 4));
          dma1(wgu + (size_t)(gks * 128 + row) * FF + gct * 64 + ch * 8, lb + LGU + (32 * lw + 8 * j) * 128);
        }
      }
    }
    unsigned vo[4], ko[4];
#pragma unroll
    for (int j = 0; j < 4; ++j) { vo[j] = g4 * 256 + 16 * ((lane & 15) ^ ((g4 << 2) | j)); ko[j] = g4 * 256 + 16 * ((lane & 15) ^ (4 * j + g4)); }
    const size_t kvbase = ((size_t)(ab * HKV + kvh) * SK + (SK - WIN) + asplit * KPW) * DH;
    const char* kb = (const char*)(p.kc + kvbase) + lw * 4096;
    const char* vb = (const char*)(p.vc + kvbase) + lw * 4096;
    if (bid < 224) VMWAIT(4); else VMWAIT(0);
    STAMP(1);
    BAR();
    BAR();
#pragma unroll
    for (int qd = 0; qd < 4; ++qd) dma4(kb + qd * 16384, ko[0], ko[1], ko[2], ko[3], lb + LK + qd * 16384 + lw * 4096);
    VMWAIT(8);
    STAMP(2);
    BAR();
    if (bid < 224) proj_tile(smc, LGU, gks * 128, lw, lane, rs, WS_G + ((gmat * 4 + gks) * 2 * 896 + gct * 32) * 16, 896 * 16, tA, tB);
#pragma unroll
    for (int qd = 0; qd < 2; ++qd) dma4(vb + qd * 16384, vo[0], vo[1], vo[2], vo[3], lb + LV + qd * 16384 + lw * 4096);
    VMWAIT(8);
    STAMP(3);
    BAR();
    BAR();
#pragma unroll
    for (int qd = 2; qd < 4; ++qd) dma4(vb + qd * 16384, vo[0], vo[1], vo[2], vo[3], lb + LV + qd * 16384 + lw * 4096);
    VMWAIT(0);
    STAMP(5);
    BAR();
    if (wave == 4 && lane < 8) {
      const int h = lane & 3;
      const LAS float* mx = reinterpret_cast<const LAS float*>(smc + S_MX);
      const LAS float* ls = reinterpret_cast<const LAS float*>(smc + S_LS);
      const float M = fmaxf(fmaxf(mx[h], mx[4 + h]), fmaxf(mx[8 + h], mx[12 + h]));
      const float L = (ls[h] + ls[4 + h]) + (ls[8 + h] + ls[12 + h]);
      st_sc1(rs, WS_ML + (((ab * HKV + kvh) * NSPLIT + asplit) * 8 + lane) * 16, v4u{tA, __float_as_uint(M), tB, __float_as_uint(L)});
    }
    BAR();
    {
#pragma unroll
      for (int j = 0; j < 4; ++j) {
        const int k = 16 * lw + 4 * j + g4, grow = (kvh * 4 + 2 * hp + (k >> 5)) * DH + 32 * dq + (k & 31);
        dma1((const char*)(p.wo + (size_t)grow * DM + 128 * cq) + (vo[j] & 255), lb + LW + (16 * lw + 4 * j) * 256);
      }
#pragma unroll
      for (int j = 0; j < 2; ++j) {
        const int k = 8 * lw + 4 * j + g4, grow = 28 * rgi + (k < 28 ? k : 27);
        dma1((const char*)(p.wd + (size_t)grow * DM + 128 * cq) + (vo[2 * (lw & 1) + j] & 255), lb + LD + (8 * lw + 4 * j) * 256);
      }
    }
    BAR();
  } else {
    if (wave < 2) {
      const v4u xa = *reinterpret_cast<const v4u*>(p.x + wave * DM + 8 * lane);
      float e[8];
#pragma unroll
      for (int k = 0; k < 4; ++k) { e[2 * k] = bflo(xa[k]); e[2 * k + 1] = bfhi(xa[k]); }
      float s = ((e[0] + e[1]) + (e[2] + e[3])) + ((e[4] + e[5]) + (e[6] + e[7]));
      s = wave_sum(s) * (1.f / DM);
      float v = 0.f;
#pragma unroll
      for (int k = 0; k < 8; ++k) { const float d = e[k] - s; v = fmaf(d, d, v); }
      v = wave_sum(v) * (1.f / DM);
      const float mean = bfr(s), den = bfr(sqrtf(bfr(bfr(v) + 1e-5f)));
      const float rc = __builtin_amdgcn_rcpf(den);
      v4u o;
#pragma unroll
      for (int k = 0; k < 4; ++k) {
        const unsigned dk = cvtpk(e[2 * k] - mean, e[2 * k + 1] - mean);
        const float d0 = bflo(dk), d1 = bfhi(dk);
        float t0 = d0 * rc, t1 = d1 * rc;
        t0 = fmaf(fmaf(-t0, den, d0), rc, t0); t1 = fmaf(fmaf(-t1, den, d1), rc, t1);
        o[k] = cvtpk(t0, t1);
      }
      *reinterpret_cast<LAS v4u*>(sm + S_XN + wave * XN_ST + lane * 16) = o;
    } else if (wave == 2) {
      if (lane < 2) *reinterpret_cast<LAS v2u*>(sm + S_H + lane * H_ST + 56) = v2u{0u, 0u};
    }
    STAMP(1);
    BAR();
    proj_tile(smc, LW, qks * 128, wave, lane, rs, WS_Q + (qks * 2048 + (qcol >> 1)) * 16, 4 * 2048 * 16, tA, tB);
    STAMP(2);
    BAR();
    BAR();
    {
      const int qoff = WS_Q + (ab * 4 * 2048 + kvh * 256 + tid) * 16;
      v4u g[4];
#pragma unroll
      for (int k = 0; k < 4; ++k) g[k] = ld_sc1(rs, qoff + k * 2048 * 16);
      if (!__all(TAGOK(g[0]) && TAGOK(g[1]) && TAGOK(g[2]) && TAGOK(g[3]))) {
#pragma nounroll
        for (unsigned spins = 0; spins < SPIN_CAP; ++spins) {
          MEM_FENCE();
#pragma unroll
          for (int k = 0; k < 4; ++k)
            if (!TAGOK(g[k])) g[k] = ld_sc1(rs, qoff + k * 2048 * 16);
          if (__all(TAGOK(g[0]) && TAGOK(g[1]) && TAGOK(g[2]) && TAGOK(g[3]))) break;
        }
      }
      const float s0 = (__uint_as_float(g[0].y) + __uint_as_float(g[1].y)) + (__uint_as_float(g[2].y) + __uint_as_float(g[3].y));
      const float s1 = (__uint_as_float(g[0].w) + __uint_as_float(g[1].w)) + (__uint_as_float(g[2].w) + __uint_as_float(g[3].w));
      *reinterpret_cast<LAS unsigned*>(sm + S_Q + wave * Q_ST + lane * 4) = cvtpk(s0, s1);
    }
    BAR();
    STAMP(3);
    float sc[4];
    {
      v4u qf[4];
#pragma unroll
      for (int s = 0; s < 4; ++s) qf[s] = lds128(smc + S_Q + (lane & 3) * Q_ST + g4 * 16 + s * 64);
      f32x4 at[4];
#pragma unroll
      for (int T = 0; T < 4; ++T) {
        at[T] = f32x4{0.f, 0.f, 0.f, 0.f};
        const int key = 64 * wave + 16 * T + (lane & 15);
#pragma unroll
        for (int s = 0; s < 4; ++s) at[T] = mfma(qf[s], lds128(smc + LK + offk(key, 4 * s + g4)), at[T]);
      }
      const f32x4 alo = (lane & 16) ? at[1] : at[0], ahi = (lane & 16) ? at[3] : at[2];
      const f32x4 acc = (lane & 32) ? ahi : alo;
      const unsigned u0 = cvtpk(acc[0], acc[1]), u1 = cvtpk(acc[2], acc[3]);
      const unsigned w0 = cvtpk(bflo(u0) * SCALE, bfhi(u0) * SCALE), w1 = cvtpk(bflo(u1) * SCALE, bfhi(u1) * SCALE);
      sc[0] = bflo(w0); sc[1] = bfhi(w0); sc[2] = bflo(w1); sc[3] = bfhi(w1);
      const float m = row16_max(tr16m(tr32m(sc[0], sc[2]), tr32m(sc[1], sc[3])));
      if ((lane & 15) == 0) *reinterpret_cast<LAS float*>(sm + S_MX + wave * 16 + g4 * 4) = m;
    }
    STAMP(11);
    BAR();
    {
      const LAS f32x4* mx = reinterpret_cast<const LAS f32x4*>(smc + S_MX);
      const f32x4 m0 = mx[0], m1 = mx[1], m2 = mx[2], m3 = mx[3];
      float pr[4];
#pragma unroll
      for (int h = 0; h < 4; ++h) {
        const float M = fmaxf(fmaxf(m0[h], m1[h]), fmaxf(m2[h], m3[h]));
        pr[h] = __builtin_amdgcn_exp2f((sc[h] - M) * LOG2E);
      }
      const unsigned u0 = cvtpk(pr[0], pr[1]), u1 = cvtpk(pr[2], pr[3]);
      LAS unsigned short* pp = reinterpret_cast<LAS unsigned short*>(sm + S_P) + 64 * wave + lane;
      pp[0] = (unsigned short)u0; pp[P_ST / 2] = (unsigned short)(u0 >> 16); pp[P_ST] = (unsigned short)u1; pp[3 * P_ST / 2] = (unsigned short)(u1 >> 16);
      const float l = row16_sum(tr16(tr32(bflo(u0), bflo(u1)), tr32(bfhi(u0), bfhi(u1))));
      if ((lane & 15) == 0) *reinterpret_cast<LAS float*>(sm + S_LS + wave * 16 + g4 * 4) = l;
    }
    STAMP(4);
    BAR();
    STAMP(5);
    {
      f32x4 a0 = {0.f, 0.f, 0.f, 0.f}, a1 = {0.f, 0.f, 0.f, 0.f};
      const LAS char* pa = smc + S_P + (lane & 3) * P_ST + g4 * 16;
#pragma unroll
      for (int ks = 0; ks < 8; ++ks) {
        const v4u a = lds128(pa + ks * 64);
        const int r0 = 32 * ks + 8 * g4 + q4, c0 = 4 * wave + (p4 >> 1), e = 8 * (p4 & 1);
        a0 = mfma(a, ldtr(smc + LV + off256(r0, c0) + e, smc + LV + off256(r0 + 4, c0) + e), a0);
        a1 = mfma(a, ldtr(smc + LV + off256(r0, c0 + 2) + e, smc + LV + off256(r0 + 4, c0 + 2) + e), a1);
      }
      asm volatile("" :: "v"(a0), "v"(a1));
      STAMP(10);
      if (lane < 16) {
        const int po = WS_PO + ((((ab * HKV + kvh) * 2) * NSPLIT + asplit) * 128 + 32 * wave + lane) * 16;
        st_sc1(rs, po, v4u{tA, __float_as_uint(a0[0]), tB, __float_as_uint(a0[1])});
        st_sc1(rs, po + 256, v4u{tA, __float_as_uint(a1[0]), tB, __float_as_uint(a1[1])});
        st_sc1(rs, po + NSPLIT * 128 * 16, v4u{tA, __float_as_uint(a0[2]), tB, __float_as_uint(a0[3])});
        st_sc1(rs, po + NSPLIT * 128 * 16 + 256, v4u{tA, __float_as_uint(a1[2]), tB, __float_as_uint(a1[3])});
      }
    }
    BAR();
    BAR();
  }
  STAMP(6);

  {
    const int cb = wave >> 2, cs = lane & 15, d0 = (wave & 3) * 8 + g4;
    const int ooff = WS_PO + ((((cb * HKV + kvh) * 2 + hp) * NSPLIT + cs) * 128 + 32 * dq + d0) * 16;
    const int moff = WS_ML + (((cb * HKV + kvh) * NSPLIT + cs) * 8 + 2 * hp) * 16;
    const int gi = tid >> 3, gb = gi >= 14, gcp = gi - 14 * gb;
    const int goff = WS_G + (((((tid >> 2) & 1) * 4 + (tid & 3)) * 2 + gb) * 896 + 14 * rgi + gcp) * 16;
    const bool gact = tid < 224;
    v4u go0 = ld_sc1(rs, ooff), go1 = ld_sc1(rs, ooff + 64), gm0 = ld_sc1(rs, moff), gm1 = ld_sc1(rs, moff + 16);
    v4u gg = v4u{tA, 0u, tB, 0u};
    if (gact) gg = ld_sc1(rs, goff);
    if (!__all(TAGOK(go0) && TAGOK(go1) && TAGOK(gm0) && TAGOK(gm1) && TAGOK(gg))) {
#pragma nounroll
      for (unsigned spins = 0; spins < SPIN_CAP; ++spins) {
        MEM_FENCE();
        if (!TAGOK(go0)) go0 = ld_sc1(rs, ooff);
        if (!TAGOK(go1)) go1 = ld_sc1(rs, ooff + 64);
        if (!TAGOK(gm0)) gm0 = ld_sc1(rs, moff);
        if (!TAGOK(gm1)) gm1 = ld_sc1(rs, moff + 16);
        if (!TAGOK(gg)) gg = ld_sc1(rs, goff);
        if (__all(TAGOK(go0) && TAGOK(go1) && TAGOK(gm0) && TAGOK(gm1) && TAGOK(gg))) break;
      }
    }
    STAMP(7);
    const float ma = __uint_as_float(gm0.y), mb = __uint_as_float(gm1.y);
    const float wa = __builtin_amdgcn_exp2f((ma - row16_max(ma)) * LOG2E), wb = __builtin_amdgcn_exp2f((mb - row16_max(mb)) * LOG2E);
    const float na = wa * __builtin_amdgcn_rcpf(row16_sum(wa * __uint_as_float(gm0.w)));
    const float nb = wb * __builtin_amdgcn_rcpf(row16_sum(wb * __uint_as_float(gm1.w)));
    const float v00 = na * __uint_as_float(go0.y), v01 = nb * __uint_as_float(go0.w), v10 = na * __uint_as_float(go1.y), v11 = nb * __uint_as_float(go1.w);
    const float ta = v00 + dpp<XOR8>(v00), tb2 = v10 + dpp<XOR8>(v10), tc = v01 + dpp<XOR8>(v01), td = v11 + dpp<XOR8>(v11);
    const float ua = (lane & 8) ? tb2 : ta, ub = (lane & 8) ? td : tc;
    const float xa = ua + dpp<XOR7>(ua), xb = ub + dpp<XOR7>(ub);
    float r = (lane & 4) ? xb : xa;
    r += dpp<XOR2>(r); r += dpp<XOR1>(r);
    if ((lane & 3) == 0)
      reinterpret_cast<LAS unsigned short*>(sm + S_O)[cb * (O_ST / 2) + ((lane >> 2) & 1) * 32 + d0 + 4 * ((lane >> 3) & 1)] = (unsigned short)cvtpk(r, r);
    if (wave < 4) {
      float s0 = __uint_as_float(gg.y), s1 = __uint_as_float(gg.w);
      s0 += dpp<XOR1>(s0); s0 += dpp<XOR2>(s0); s1 += dpp<XOR1>(s1); s1 += dpp<XOR2>(s1);
      const unsigned own = cvtpk(s0, s1), oth = cvtpk(dpp<XOR7>(s0), dpp<XOR7>(s1));
      const float g0 = bflo(own), g1 = bfhi(own);
      const unsigned sg = cvtpk(g0 * __builtin_amdgcn_rcpf(1.f + __expf(-g0)), g1 * __builtin_amdgcn_rcpf(1.f + __expf(-g1)));
      const unsigned hh = cvtpk(bflo(sg) * bflo(oth), bfhi(sg) * bfhi(oth));
      if (gact && (lane & 7) == 0) *reinterpret_cast<LAS unsigned*>(sm + S_H + gb * H_ST + gcp * 4) = hh;
    }
  }
  VMWAIT(0);
  BAR();
  {
    const LAS char* oa = smc + S_O + (lane & 1) * O_ST + g4 * 16;
    const int r0 = 8 * g4 + q4, c0 = 2 * wave + (p4 >> 1), e = 8 * (p4 & 1);
    f32x4 fa = {0.f, 0.f, 0.f, 0.f}, fb = {0.f, 0.f, 0.f, 0.f};
    fa = mfma(lds128(oa), ldtr(smc + LW + off256(r0, c0) + e, smc + LW + off256(r0 + 4, c0) + e), fa);
    fa = mfma(lds128(oa + 64), ldtr(smc + LW + off256(r0 + 32, c0) + e, smc + LW + off256(r0 + 36, c0) + e), fa);
    fb = mfma(lds128(smc + S_H + (lane & 1) * H_ST + g4 * 16), ldtr(smc + LD + off256(r0, c0) + e, smc + LD + off256(r0 + 4, c0) + e), fb);
    if (lane < 16) {
      const int fo = WS_F + (rgi * 1024 + 128 * cq + 16 * wave + lane) * 16;
      st_sc1(rs, fo, v4u{tA, __float_as_uint(fa[0]), tB, __float_as_uint(fb[0])});
      st_sc1(rs, fo + 512 * 16, v4u{tA, __float_as_uint(fa[1]), tB, __float_as_uint(fb[1])});
    }
  }
  STAMP(8);

  if (wave == 0) {
    const int ob = (xcd * 32 + idx) * 4;
    const unsigned short xfin = p.x[ob + g4];
    v4u g[4];
    const int o0 = WS_F + (lane * 1024 + ob) * 16;
#pragma unroll
    for (int k = 0; k < 4; ++k) g[k] = ld_sc1(rs, o0 + k * 16);
    if (!__all(TAGOK(g[0]) && TAGOK(g[1]) && TAGOK(g[2]) && TAGOK(g[3]))) {
#pragma nounroll
      for (unsigned spins = 0; spins < SPIN_CAP; ++spins) {
        MEM_FENCE();
#pragma unroll
        for (int k = 0; k < 4; ++k)
          if (!TAGOK(g[k])) g[k] = ld_sc1(rs, o0 + k * 16);
        if (__all(TAGOK(g[0]) && TAGOK(g[1]) && TAGOK(g[2]) && TAGOK(g[3]))) break;
      }
    }
    STAMP(9);
    float va = tr16(tr32(__uint_as_float(g[0].y), __uint_as_float(g[2].y)), tr32(__uint_as_float(g[1].y), __uint_as_float(g[3].y)));
    float vb = tr16(tr32(__uint_as_float(g[0].w), __uint_as_float(g[2].w)), tr32(__uint_as_float(g[1].w), __uint_as_float(g[3].w)));
    va = row16_sum(va); vb = row16_sum(vb);
    if ((lane & 15) == 0) {
      const float r = bfr(bflo(xfin) + bfr(va)) + bfr(vb);
      p.out[ob + g4] = (unsigned short)cvtpk(r, r);
      if (bid == 0 && lane == 0) *reinterpret_cast<volatile u64*>(p.ws) = epoch + 1ull;
    }
  }
  STAMP_FLUSH;
}

extern "C" void kernel_launch(void* const* d_in, const int* in_sizes, int n_in, void* d_out, int out_size, void* d_ws,
                              size_t ws_size, hipStream_t stream) {
  Params p;
  p.x = (const unsigned short*)d_in[0];
  p.kc = (const unsigned short*)d_in[1];
  p.vc = (const unsigned short*)d_in[2];
  p.wq = (const unsigned short*)d_in[3];
  p.wo = (const unsigned short*)d_in[4];
  p.wg = (const unsigned short*)d_in[6];
  p.wu = (const unsigned short*)d_in[7];
  p.wd = (const unsigned short*)d_in[8];
  p.out = (unsigned short*)d_out;
  p.ws = (unsigned char*)d_ws;
  (void)hipFuncSetAttribute((const void*)fused_decode_kernel, hipFuncAttributeMaxDynamicSharedMemorySize, LDS_BYTES);
  hipLaunchKernelGGL(fused_decode_kernel, dim3(NWG), dim3(NTHR), LDS_BYTES, stream, p);
}
```

```cpp
#include <hip/hip_runtime.h>
#include <hip/hip_bf16.h>
#include <stdint.h>

typedef unsigned v4u __attribute__((ext_vector_type(4)));
typedef unsigned v2u __attribute__((ext_vector_type(2)));
typedef float f32x4 __attribute__((ext_vector_type(4)));
typedef float f32x2 __attribute__((ext_vector_type(2)));
typedef short bf16x8 __attribute__((ext_vector_type(8)));
typedef short s16x4 __attribute__((ext_vector_type(4)));
typedef unsigned long long u64;
#define LAS __attribute__((address_space(3)))

constexpr int DM = 512, HQ = 32, HKV = 8, SK = 32768, DH = 128, WIN = 4096, FF = 1792;
constexpr int NSPLIT = 16, KPW = WIN / NSPLIT;
constexpr int NWG = 256, NTHR = 512;
constexpr unsigned SPIN_CAP = 1u << 18;
#define D3A 8
#define D3B 3
#define D4A 24
#define D4B 1

constexpr int WS_Q = 4096;
constexpr int WS_G = WS_Q + 2 * 4 * 2048 * 16;
constexpr int WS_ML = WS_G + 16 * 896 * 16;
constexpr int WS_PO = WS_ML + 32768;
constexpr int WS_F = WS_PO + 1048576;
constexpr int WS_END = WS_F + 1048576;

constexpr int LK = 0;
constexpr int LV = 65536;
constexpr int LGU = LV + 49152;
constexpr int LW = 131072;
constexpr int LD = 147456;
constexpr int LS = 155648;
constexpr int S_XN = LS;
constexpr int XN_ST = 1040;
constexpr int S_Q = S_XN + 2 * XN_ST;
constexpr int Q_ST = 272;
constexpr int S_P = S_Q + 4 * Q_ST;
constexpr int P_ST = 528;
constexpr int S_MX = S_P + 4 * P_ST;
constexpr int S_LS = S_MX + 64;
constexpr int S_O = S_LS + 64;
constexpr int O_ST = 144;
constexpr int S_H = S_O + 2 * O_ST;
constexpr int H_ST = 80;
constexpr int LDS_BYTES = S_H + 2 * H_ST;

struct Params {
  const unsigned short* x;
  const unsigned short* kc;
  const unsigned short* vc;
  const unsigned short* wq;
  const unsigned short* wo;
  const unsigned short* wg;
  const unsigned short* wu;
  const unsigned short* wd;
  unsigned short* out;
  unsigned char* ws;
};

__device__ __forceinline__ float bflo(unsigned u) { return __builtin_bit_cast(float, u << 16); }
__device__ __forceinline__ float bfhi(unsigned u) { return __builtin_bit_cast(float, u & 0xffff0000u); }
typedef __bf16 bf16x2 __attribute__((ext_vector_type(2)));
__device__ __forceinline__ unsigned cvtpk(float lo, float hi) {
  return __builtin_bit_cast(unsigned, __builtin_convertvector((f32x2){lo, hi}, bf16x2));
}
__device__ __forceinline__ float bfr(float x) { return bflo(cvtpk(x, x)); }
template <int CTRL> __device__ __forceinline__ float dpp(float x) {
  return __builtin_bit_cast(float, __builtin_amdgcn_mov_dpp(__builtin_bit_cast(int, x), CTRL, 0xf, 0xf, true));
}
constexpr int XOR1 = 0xB1, XOR2 = 0x4E, XOR7 = 0x141, MIRR = 0x140, XOR8 = 0x128;
__device__ __forceinline__ float tr32(float a, float b) {
  auto s = __builtin_amdgcn_permlane32_swap(__float_as_uint(a), __float_as_uint(b), false, false);
  return __uint_as_float(s[0]) + __uint_as_float(s[1]);
}
__device__ __forceinline__ float tr16(float a, float b) {
  auto s = __builtin_amdgcn_permlane16_swap(__float_as_uint(a), __float_as_uint(b), false, false);
  return __uint_as_float(s[0]) + __uint_as_float(s[1]);
}
__device__ __forceinline__ float tr32m(float a, float b) {
  auto s = __builtin_amdgcn_permlane32_swap(__float_as_uint(a), __float_as_uint(b), false, false);
  return fmaxf(__uint_as_float(s[0]), __uint_as_float(s[1]));
}
__device__ __forceinline__ float tr16m(float a, float b) {
  auto s = __builtin_amdgcn_permlane16_swap(__float_as_uint(a), __float_as_uint(b), false, false);
  return fmaxf(__uint_as_float(s[0]), __uint_as_float(s[1]));
}
__device__ __forceinline__ float row16_sum(float x) {
  x += dpp<XOR1>(x); x += dpp<XOR2>(x); x += dpp<XOR7>(x); x += dpp<MIRR>(x);
  return x;
}
__device__ __forceinline__ float row16_max(float x) {
  x = fmaxf(x, dpp<XOR1>(x)); x = fmaxf(x, dpp<XOR2>(x)); x = fmaxf(x, dpp<XOR7>(x)); x = fmaxf(x, dpp<MIRR>(x));
  return x;
}
__device__ __forceinline__ float wave_sum(float x) {
  x = row16_sum(x);
  x = tr16(x, x);
  x = tr32(x, x);
  return x;
}
__device__ __forceinline__ v4u ld_sc1(__amdgpu_buffer_rsrc_t rs, int off) { return __builtin_amdgcn_raw_buffer_load_b128(rs, off, 0, 16); }
__device__ __forceinline__ void st_sc1(__amdgpu_buffer_rsrc_t rs, int off, v4u v) { __builtin_amdgcn_raw_buffer_store_b128(v, rs, off, 0, 16); }
__device__ __forceinline__ f32x4 mfma(v4u a, v4u b, f32x4 c) {
  return __builtin_amdgcn_mfma_f32_16x16x32_bf16(__builtin_bit_cast(bf16x8, a), __builtin_bit_cast(bf16x8, b), c, 0, 0, 0);
}
__device__ __forceinline__ v4u lds128(const LAS char* p) { return *reinterpret_cast<const LAS v4u*>(p); }
__device__ __forceinline__ v4u ldtr(const LAS char* a0, const LAS char* a1) {
  const s16x4 lo = __builtin_amdgcn_ds_read_tr16_b64_v4i16((LAS s16x4*)a0);
  const s16x4 hi = __builtin_amdgcn_ds_read_tr16_b64_v4i16((LAS s16x4*)a1);
  const v2u l2 = __builtin_bit_cast(v2u, lo), h2 = __builtin_bit_cast(v2u, hi);
  return v4u{l2.x, l2.y, h2.x, h2.y};
}
__device__ __forceinline__ int off256(int row, int ch) { return 256 * row + 16 * (ch ^ (((row & 3) << 2) | ((row >> 2) & 3))); }
__device__ __forceinline__ int offk(int row, int ch) { return 256 * row + 16 * (ch ^ (row & 15)); }
__device__ __forceinline__ int off128(int row, int ch) { return 128 * row + 16 * (ch ^ ((row & 2) | ((row >> 1) & 4))); }
__device__ __forceinline__ void dma1(const void* g, unsigned lds_dst) {
  unsigned keep;
  asm volatile("s_mov_b32 %0, m0\n\ts_mov_b32 m0, %2\n\ts_nop 0\n\tglobal_load_lds_dwordx4 %1, off nt\n\ts_mov_b32 m0, %0"
               : "=&s"(keep) : "v"(g), "s"(lds_dst) : "memory");
}
__device__ __forceinline__ void dma4(const void* sbase, unsigned v0, unsigned v1, unsigned v2, unsigned v3, unsigned lds_dst) {
  unsigned keep;
  asm volatile("s_mov_b32 %0, m0\n\ts_mov_b32 m0, %6\n\ts_nop 0\n\t"
               "global_load_lds_dwordx4 %1, %5 nt\n\t"
               "global_load_lds_dwordx4 %2, %5 offset:1024 nt\n\t"
               "global_load_lds_dwordx4 %3, %5 offset:2048 nt\n\t"
               "global_load_lds_dwordx4 %4, %5 offset:3072 nt\n\t"
               "s_mov_b32 m0, %0"
               : "=&s"(keep) : "v"(v0), "v"(v1), "v"(v2), "v"(v3), "s"(sbase), "s"(lds_dst) : "memory");
}
#define STAMP(k) do {} while (0)
#define STAMP_DECL do {} while (0)
#define STAMP_FLUSH do {} while (0)
#define MEM_FENCE() asm volatile("" ::: "memory")
#define BAR() asm volatile("s_waitcnt lgkmcnt(0)\n\ts_barrier" ::: "memory")
#define VMWAIT(n) asm volatile("s_waitcnt vmcnt(" #n ")" ::: "memory")
#define TAGOK(g) ((g).x == tA && (g).z == tB)

constexpr float LOG2E = 1.4426950408889634f;
constexpr float SCALE = 0.08838834764831845f;

__device__ __forceinline__ void proj_tile(const LAS char* sm, int tile, int kbase, int c, int lane, __amdgpu_buffer_rsrc_t rs,
                                          int dst, int bstride, unsigned tA, unsigned tB) {
  const int g = lane >> 4, q = (lane & 15) >> 2, pp = lane & 3;
  const LAS char* ap = sm + S_XN + (lane & 1) * XN_ST + (kbase + 8 * g) * 2;
  f32x4 acc = {0.f, 0.f, 0.f, 0.f};
#pragma unroll
  for (int ks = 0; ks < 4; ++ks) {
    const int r0 = 32 * ks + 8 * g + q;
    const v4u b = ldtr(sm + tile + off128(r0, 2 * c + (pp >> 1)) + 8 * (pp & 1), sm + tile + off128(r0 + 4, 2 * c + (pp >> 1)) + 8 * (pp & 1));
    acc = mfma(lds128(ap + ks * 64), b, acc);
  }
  const float n0 = dpp<XOR1>(acc[0]), n1 = dpp<XOR1>(acc[1]);
  const bool odd = lane & 1;
  const v4u gr = {tA, __float_as_uint(odd ? n1 : acc[0]), tB, __float_as_uint(odd ? acc[1] : n0)};
  if (lane < 16) st_sc1(rs, dst + (odd ? bstride : 0) + (c * 8 + (lane >> 1)) * 16, gr);
}

__global__ void __launch_bounds__(NTHR, 2) fused_decode_kernel(Params p) {
  extern __shared__ __attribute__((aligned(16))) char smem_[];
  const LAS char* smc = (const LAS char*)smem_;
  LAS char* sm = (LAS char*)smem_;
  const unsigned lb = (unsigned)(size_t)sm;
  const int tid = threadIdx.x, lane = tid & 63, wave = __builtin_amdgcn_readfirstlane(tid >> 6);
  const int bid = blockIdx.x, xcd = bid & 7, idx = bid >> 3;
  const __amdgpu_buffer_rsrc_t rs = __builtin_amdgcn_make_buffer_rsrc(p.ws, 0, WS_END, 0x00020000);
  STAMP_DECL;
  STAMP(0);
  const u64 epoch = *reinterpret_cast<const __attribute__((address_space(4))) u64*>(reinterpret_cast<uintptr_t>(p.ws));
  const u64 tag64 = (epoch + 1ull) * 0x9E3779B97F4A7C15ull;
  const unsigned tA = (unsigned)tag64, tB = (unsigned)(tag64 >> 32);

  const int qks = idx >> 3, qcol = xcd * 512 + (idx & 7) * 64;
  const int gtile = bid < 224 ? bid : 0, gmat = gtile / 112, gct = (gtile % 112) >> 2, gks = gtile & 3;
  const int ab = idx >> 4, asplit = idx & 15, kvh = xcd;
  const int rgi = xcd * 8 + (idx >> 2), cq = idx & 3, hp = (rgi >> 2) & 1, dq = rgi & 3;
  const int g4 = lane >> 4, q4 = (lane & 15) >> 2, p4 = lane & 3;

  if (wave >= 4) {
    const int lw = wave - 4;
    unsigned vo[4], ko[4];
#pragma unroll
    for (int j = 0; j < 4; ++j) { vo[j] = g4 * 256 + 16 * ((lane & 15) ^ ((g4 << 2) | j)); ko[j] = g4 * 256 + 16 * ((lane & 15) ^ (4 * j + g4)); }
    const size_t kvbase = ((size_t)(ab * HKV + kvh) * SK + (SK - WIN) + asplit * KPW) * DH;
    const char* kb = (const char*)(p.kc + kvbase) + lw * 4096;
    const char* vb = (const char*)(p.vc + kvbase) + lw * 4096;
#define KDMA(qd) dma4(kb + (qd) * 16384, ko[0], ko[1], ko[2], ko[3], lb + LK + (qd) * 16384 + lw * 4096)
#define VDMA(qd) dma4(vb + (qd) * 16384, vo[0], vo[1], vo[2], vo[3], lb + LV + (qd) * 16384 + lw * 4096)
    {
      const int r = 32 * lw + (lane >> 3);
#pragma unroll
      for (int j = 0; j < 4; ++j) {
        const int row = r + 8 * j, ch = (lane & 7) ^ ((row & 2) | ((row >> 1) & 4));
        dma1(p.wq + (size_t)(qks * 128 + row) * (HQ * DH) + qcol + ch * 8, lb + LW + (32 * lw + 8 * j) * 128);
      }
      KDMA(0);
      const unsigned short* wgu = gmat ? p.wu : p.wg;
      if (bid < 224) {
#pragma unroll
        for (int j = 0; j < 4; ++j) {
          const int row = r + 8 * j, ch = (lane & 7) ^ ((row & 2) | ((row >> 1) & 4));
          dma1(wgu + (size_t)(gks * 128 + row) * FF + gct * 64 + ch * 8, lb + LGU + (32 * lw + 8 * j) * 128);
        }
        VMWAIT(8);
      } else {
        VMWAIT(4);
      }
    }
    STAMP(1);
    BAR();
    BAR();
    KDMA(1); KDMA(2); KDMA(3);
    VMWAIT(8);
    STAMP(2);
    BAR();
    if (bid < 224) proj_tile(smc, LGU, gks * 128, lw, lane, rs, WS_G + ((gmat * 4 + gks) * 2 * 896 + gct * 32) * 16, 896 * 16, tA, tB);
    VDMA(0); VDMA(1); VDMA(2);
    VMWAIT(12);
    STAMP(3);
    BAR();
    VDMA(3);
    BAR();
    VMWAIT(0);
    STAMP(5);
    BAR();
    if (wave == 4 && lane < 8) {
      const int h = lane & 3;
      const LAS float* mx = reinterpret_cast<const LAS float*>(smc + S_MX);
      const LAS float* ls = reinterpret_cast<const LAS float*>(smc + S_LS);
      const float M = fmaxf(fmaxf(mx[h], mx[4 + h]), fmaxf(mx[8 + h], mx[12 + h]));
      const float L = (ls[h] + ls[4 + h]) + (ls[8 + h] + ls[12 + h]);
      st_sc1(rs, WS_ML + (((ab * HKV + kvh) * NSPLIT + asplit) * 8 + lane) * 16, v4u{tA, __float_as_uint(M), tB, __float_as_uint(L)});
    }
    BAR();
    {
#pragma unroll
      for (int j = 0; j < 4; ++j) {
        const int k = 16 * lw + 4 * j + g4, grow = (kvh * 4 + 2 * hp + (k >> 5)) * DH + 32 * dq + (k & 31);
        dma1((const char*)(p.wo + (size_t)grow * DM + 128 * cq) + (vo[j] & 255), lb + LW + (16 * lw + 4 * j) * 256);
      }
#pragma unroll
      for (int j = 0; j < 2; ++j) {
        const int k = 8 * lw + 4 * j + g4, grow = 28 * rgi + (k < 28 ? k : 27);
        dma1((const char*)(p.wd + (size_t)grow * DM + 128 * cq) + (vo[2 * (lw & 1) + j] & 255), lb + LD + (8 * lw + 4 * j) * 256);
      }
    }
    VMWAIT(0);
    BAR();
    v4u bw[2][2];
    f32x4 fb[2];
    {
      const int r0 = 8 * g4 + q4, e = 8 * (p4 & 1);
      const v4u ha = lds128(smc + S_H + (lane & 1) * H_ST + g4 * 16);
#pragma unroll
      for (int t = 0; t < 2; ++t) {
        const int c0 = 4 * lw + 2 * t + (p4 >> 1);
        bw[t][0] = ldtr(smc + LW + off256(r0, c0) + e, smc + LW + off256(r0 + 4, c0) + e);
        bw[t][1] = ldtr(smc + LW + off256(r0 + 32, c0) + e, smc + LW + off256(r0 + 36, c0) + e);
        fb[t] = mfma(ha, ldtr(smc + LD + off256(r0, c0) + e, smc + LD + off256(r0 + 4, c0) + e), f32x4{0.f, 0.f, 0.f, 0.f});
      }
    }
    const int fo = WS_F + (rgi * 1024 + 128 * cq + 32 * lw + lane) * 16;
    const LAS char* oa = smc + S_O + (lane & 1) * O_ST + g4 * 16;
    STAMP(7);
    BAR();
    {
      const v4u o0 = lds128(oa), o1 = lds128(oa + 64);
#pragma unroll
      for (int t = 0; t < 2; ++t) {
        f32x4 fa = mfma(o0, bw[t][0], f32x4{0.f, 0.f, 0.f, 0.f});
        fa = mfma(o1, bw[t][1], fa);
        if (lane < 16) {
          st_sc1(rs, fo + t * 256, v4u{tA, __float_as_uint(fa[0]), tB, __float_as_uint(fb[t][0])});
          st_sc1(rs, fo + t * 256 + 512 * 16, v4u{tA, __float_as_uint(fa[1]), tB, __float_as_uint(fb[t][1])});
        }
      }
    }
    STAMP(8);
  } else {
    if (wave < 2) {
      const v4u xa = *reinterpret_cast<const v4u*>(p.x + wave * DM + 8 * lane);
      float e[8];
#pragma unroll
      for (int k = 0; k < 4; ++k) { e[2 * k] = bflo(xa[k]); e[2 * k + 1] = bfhi(xa[k]); }
      float s = ((e[0] + e[1]) + (e[2] + e[3])) + ((e[4] + e[5]) + (e[6] + e[7]));
      s = wave_sum(s) * (1.f / DM);
      float v = 0.f;
#pragma unroll
      for (int k = 0; k < 8; ++k) { const float d = e[k] - s; v = fmaf(d, d, v); }
      v = wave_sum(v) * (1.f / DM);
      const float mean = bfr(s), den = bfr(sqrtf(bfr(bfr(v) + 1e-5f)));
      const float rc = __builtin_amdgcn_rcpf(den);
      v4u o;
#pragma unroll
      for (int k = 0; k < 4; ++k) {
        const unsigned dk = cvtpk(e[2 * k] - mean, e[2 * k + 1] - mean);
        const float d0 = bflo(dk), d1 = bfhi(dk);
        float t0 = d0 * rc, t1 = d1 * rc;
        t0 = fmaf(fmaf(-t0, den, d0), rc, t0); t1 = fmaf(fmaf(-t1, den, d1), rc, t1);
        o[k] = cvtpk(t0, t1);
      }
      *reinterpret_cast<LAS v4u*>(sm + S_XN + wave * XN_ST + lane * 16) = o;
    } else if (wave == 2) {
      if (lane < 2) *reinterpret_cast<LAS v2u*>(sm + S_H + lane * H_ST + 56) = v2u{0u, 0u};
    }
    STAMP(1);
    BAR();
    proj_tile(smc, LW, qks * 128, wave, lane, rs, WS_Q + (qks * 2048 + (qcol >> 1)) * 16, 4 * 2048 * 16, tA, tB);
    STAMP(2);
    BAR();
    BAR();
    {
      const int qoff = WS_Q + (ab * 4 * 2048 + kvh * 256 + tid) * 16;
      v4u g[4];
#pragma unroll
      for (int k = 0; k < 4; ++k) g[k] = ld_sc1(rs, qoff + k * 2048 * 16);
      if (!__all(TAGOK(g[0]) && TAGOK(g[1]) && TAGOK(g[2]) && TAGOK(g[3]))) {
#pragma nounroll
        for (unsigned spins = 0; spins < SPIN_CAP; ++spins) {
          MEM_FENCE();
#pragma unroll
          for (int k = 0; k < 4; ++k)
            if (!TAGOK(g[k])) g[k] = ld_sc1(rs, qoff + k * 2048 * 16);
          if (__all(TAGOK(g[0]) && TAGOK(g[1]) && TAGOK(g[2]) && TAGOK(g[3]))) break;
        }
      }
      const float s0 = (__uint_as_float(g[0].y) + __uint_as_float(g[1].y)) + (__uint_as_float(g[2].y) + __uint_as_float(g[3].y));
      const float s1 = (__uint_as_float(g[0].w) + __uint_as_float(g[1].w)) + (__uint_as_float(g[2].w) + __uint_as_float(g[3].w));
      *reinterpret_cast<LAS unsigned*>(sm + S_Q + wave * Q_ST + lane * 4) = cvtpk(s0, s1);
    }
    const int gi = tid >> 3, gb = gi >= 14, gcp = gi - 14 * gb;
    const int goff = WS_G + (((((tid >> 2) & 1) * 4 + (tid & 3)) * 2 + gb) * 896 + 14 * rgi + gcp) * 16;
    const bool gact = tid < 224;
    v4u gg = v4u{tA, 0u, tB, 0u};
    if (gact) gg = ld_sc1(rs, goff);
    BAR();
    STAMP(3);
    float sc[4];
    {
      v4u qf[4];
#pragma unroll
      for (int s = 0; s < 4; ++s) qf[s] = lds128(smc + S_Q + (lane & 3) * Q_ST + g4 * 16 + s * 64);
      f32x4 at[4];
#pragma unroll
      for (int T = 0; T < 4; ++T) {
        at[T] = f32x4{0.f, 0.f, 0.f, 0.f};
        const int key = 64 * wave + 16 * T + (lane & 15);
#pragma unroll
        for (int s = 0; s < 4; ++s) at[T] = mfma(qf[s], lds128(smc + LK + offk(key, 4 * s + g4)), at[T]);
      }
      const f32x4 alo = (lane & 16) ? at[1] : at[0], ahi = (lane & 16) ? at[3] : at[2];
      const f32x4 acc = (lane & 32) ? ahi : alo;
      const unsigned u0 = cvtpk(acc[0], acc[1]), u1 = cvtpk(acc[2], acc[3]);
      const unsigned w0 = cvtpk(bflo(u0) * SCALE, bfhi(u0) * SCALE), w1 = cvtpk(bflo(u1) * SCALE, bfhi(u1) * SCALE);
      sc[0] = bflo(w0); sc[1] = bfhi(w0); sc[2] = bflo(w1); sc[3] = bfhi(w1);
      const float m = row16_max(tr16m(tr32m(sc[0], sc[2]), tr32m(sc[1], sc[3])));
      if ((lane & 15) == 0) *reinterpret_cast<LAS float*>(sm + S_MX + wave * 16 + g4 * 4) = m;
    }
    STAMP(11);
    BAR();
    {
      const LAS f32x4* mx = reinterpret_cast<const LAS f32x4*>(smc + S_MX);
      const f32x4 m0 = mx[0], m1 = mx[1], m2 = mx[2], m3 = mx[3];
      float pr[4];
#pragma unroll
      for (int h = 0; h < 4; ++h) {
        const float M = fmaxf(fmaxf(m0[h], m1[h]), fmaxf(m2[h], m3[h]));
        pr[h] = __builtin_amdgcn_exp2f((sc[h] - M) * LOG2E);
      }
      const unsigned u0 = cvtpk(pr[0], pr[1]), u1 = cvtpk(pr[2], pr[3]);
      LAS unsigned short* pp = reinterpret_cast<LAS unsigned short*>(sm + S_P) + 64 * wave + lane;
      pp[0] = (unsigned short)u0; pp[P_ST / 2] = (unsigned short)(u0 >> 16); pp[P_ST] = (unsigned short)u1; pp[3 * P_ST / 2] = (unsigned short)(u1 >> 16);
      const float l = row16_sum(tr16(tr32(bflo(u0), bflo(u1)), tr32(bfhi(u0), bfhi(u1))));
      if ((lane & 15) == 0) *reinterpret_cast<LAS float*>(sm + S_LS + wave * 16 + g4 * 4) = l;
    }
    {
      if (!__all(TAGOK(gg))) {
#pragma nounroll
        for (unsigned spins = 0; spins < SPIN_CAP; ++spins) {
          MEM_FENCE();
          if (!TAGOK(gg)) gg = ld_sc1(rs, goff);
          if (__all(TAGOK(gg))) break;
        }
      }
      float s0 = __uint_as_float(gg.y), s1 = __uint_as_float(gg.w);
      s0 += dpp<XOR1>(s0); s0 += dpp<XOR2>(s0); s1 += dpp<XOR1>(s1); s1 += dpp<XOR2>(s1);
      const unsigned own = cvtpk(s0, s1), oth = cvtpk(dpp<XOR7>(s0), dpp<XOR7>(s1));
      const float g0 = bflo(own), g1 = bfhi(own);
      const unsigned sg = cvtpk(g0 * __builtin_amdgcn_rcpf(1.f + __expf(-g0)), g1 * __builtin_amdgcn_rcpf(1.f + __expf(-g1)));
      const unsigned hh = cvtpk(bflo(sg) * bflo(oth), bfhi(sg) * bfhi(oth));
      if (gact && (lane & 7) == 0) *reinterpret_cast<LAS unsigned*>(sm + S_H + gb * H_ST + gcp * 4) = hh;
    }
    STAMP(4);
    BAR();
    STAMP(5);
    {
      f32x4 a0 = {0.f, 0.f, 0.f, 0.f}, a1 = {0.f, 0.f, 0.f, 0.f};
      const LAS char* pa = smc + S_P + (lane & 3) * P_ST + g4 * 16;
#pragma unroll
      for (int ks = 0; ks < 8; ++ks) {
        const v4u a = lds128(pa + ks * 64);
        const int r0 = 32 * ks + 8 * g4 + q4, c0 = 4 * wave + (p4 >> 1), e = 8 * (p4 & 1);
        a0 = mfma(a, ldtr(smc + LV + off256(r0, c0) + e, smc + LV + off256(r0 + 4, c0) + e), a0);
        a1 = mfma(a, ldtr(smc + LV + off256(r0, c0 + 2) + e, smc + LV + off256(r0 + 4, c0 + 2) + e), a1);
      }
      asm volatile("" :: "v"(a0), "v"(a1));
      STAMP(10);
      if (lane < 16) {
        const int po = WS_PO + ((((ab * HKV + kvh) * 2) * NSPLIT + asplit) * 128 + 32 * wave + lane) * 16;
        st_sc1(rs, po, v4u{tA, __float_as_uint(a0[0]), tB, __float_as_uint(a0[1])});
        st_sc1(rs, po + 256, v4u{tA, __float_as_uint(a1[0]), tB, __float_as_uint(a1[1])});
        st_sc1(rs, po + NSPLIT * 128 * 16, v4u{tA, __float_as_uint(a0[2]), tB, __float_as_uint(a0[3])});
        st_sc1(rs, po + NSPLIT * 128 * 16 + 256, v4u{tA, __float_as_uint(a1[2]), tB, __float_as_uint(a1[3])});
      }
    }
    BAR();
    STAMP(6);
    BAR();
    {
      const int cb = wave >> 1, cs = lane & 15, d0 = (wave & 1) * 16 + g4;
      const int ooff = WS_PO + ((((cb * HKV + kvh) * 2 + hp) * NSPLIT + cs) * 128 + 32 * dq + d0) * 16;
      const int moff = WS_ML + (((cb * HKV + kvh) * NSPLIT + cs) * 8 + 2 * hp) * 16;
      LAS unsigned short* od = reinterpret_cast<LAS unsigned short*>(sm + S_O) + cb * (O_ST / 2) + ((lane >> 1) & 1) * 32 + d0 + 4 * ((lane >> 2) & 3);
      __builtin_amdgcn_s_sleep(D3A);
      v4u go[4], gm0, gm1;
#pragma unroll
      for (int j = 0; j < 4; ++j) go[j] = ld_sc1(rs, ooff + 64 * j);
      gm0 = ld_sc1(rs, moff); gm1 = ld_sc1(rs, moff + 16);
#define ALLOK (TAGOK(go[0]) && TAGOK(go[1]) && TAGOK(go[2]) && TAGOK(go[3]) && TAGOK(gm0) && TAGOK(gm1))
      if (!__all(ALLOK)) {
#pragma nounroll
        for (unsigned spins = 0; spins < SPIN_CAP; ++spins) {
          __builtin_amdgcn_s_sleep(D3B);
          MEM_FENCE();
#pragma unroll
          for (int j = 0; j < 4; ++j)
            if (!TAGOK(go[j])) go[j] = ld_sc1(rs, ooff + 64 * j);
          if (!TAGOK(gm0)) gm0 = ld_sc1(rs, moff);
          if (!TAGOK(gm1)) gm1 = ld_sc1(rs, moff + 16);
          if (__all(ALLOK)) break;
        }
      }
#undef ALLOK
      STAMP(7);
      const float ma = __uint_as_float(gm0.y), mb = __uint_as_float(gm1.y);
      const float wa = __builtin_amdgcn_exp2f((ma - row16_max(ma)) * LOG2E), wb = __builtin_amdgcn_exp2f((mb - row16_max(mb)) * LOG2E);
      const float na = wa * __builtin_amdgcn_rcpf(row16_sum(wa * __uint_as_float(gm0.w)));
      const float nb = wb * __builtin_amdgcn_rcpf(row16_sum(wb * __uint_as_float(gm1.w)));
      float v[4][2];
#pragma unroll
      for (int j = 0; j < 4; ++j) { v[j][0] = na * __uint_as_float(go[j].y); v[j][1] = nb * __uint_as_float(go[j].w); }
      float u[2][2], w2[2];
#pragma unroll
      for (int j = 0; j < 2; ++j)
#pragma unroll
        for (int h = 0; h < 2; ++h) { const float a = v[j][h] + dpp<XOR8>(v[j][h]), b = v[j + 2][h] + dpp<XOR8>(v[j + 2][h]); u[j][h] = (lane & 8) ? b : a; }
#pragma unroll
      for (int h = 0; h < 2; ++h) { const float a = u[0][h] + dpp<XOR7>(u[0][h]), b = u[1][h] + dpp<XOR7>(u[1][h]); w2[h] = (lane & 4) ? b : a; }
      const float a = w2[0] + dpp<XOR2>(w2[0]), b = w2[1] + dpp<XOR2>(w2[1]);
      float r = (lane & 2) ? b : a;
      r += dpp<XOR1>(r);
      if ((lane & 1) == 0) *od = (unsigned short)cvtpk(r, r);
    }
    BAR();
    STAMP(8);
  }

  if (wave == 0) {
    const int ob = (xcd * 32 + idx) * 4;
    const unsigned short xfin = p.x[ob + g4];
    v4u g[4];
    const int o0 = WS_F + (lane * 1024 + ob) * 16;
    __builtin_amdgcn_s_sleep(D4A);
#pragma unroll
    for (int k = 0; k < 4; ++k) g[k] = ld_sc1(rs, o0 + k * 16);
    if (!__all(TAGOK(g[0]) && TAGOK(g[1]) && TAGOK(g[2]) && TAGOK(g[3]))) {
#pragma nounroll
      for (unsigned spins = 0; spins < SPIN_CAP; ++spins) {
        __builtin_amdgcn_s_sleep(D4B);
        MEM_FENCE();
#pragma unroll
        for (int k = 0; k < 4; ++k)
          if (!TAGOK(g[k])) g[k] = ld_sc1(rs, o0 + k * 16);
        if (__all(TAGOK(g[0]) && TAGOK(g[1]) && TAGOK(g[2]) && TAGOK(g[3]))) break;
      }
    }
    STAMP(9);
    float va = tr16(tr32(__uint_as_float(g[0].y), __uint_as_float(g[2].y)), tr32(__uint_as_float(g[1].y), __uint_as_float(g[3].y)));
    float vb = tr16(tr32(__uint_as_float(g[0].w), __uint_as_float(g[2].w)), tr32(__uint_as_float(g[1].w), __uint_as_float(g[3].w)));
    va = row16_sum(va); vb = row16_sum(vb);
    if ((lane & 15) == 0) {
      const float r = bfr(bflo(xfin) + bfr(va)) + bfr(vb);
      p.out[ob + g4] = (unsigned short)cvtpk(r, r);
      if (bid == 0 && lane == 0) *reinterpret_cast<volatile u64*>(p.ws) = epoch + 1ull;
    }
  }
  STAMP_FLUSH;
}

extern "C" void kernel_launch(void* const* d_in, const int* in_sizes, int n_in, void* d_out, int out_size, void* d_ws,
                              size_t ws_size, hipStream_t stream) {
  Params p;
  p.x = (const unsigned short*)d_in[0];
  p.kc = (const unsigned short*)d_in[1];
  p.vc = (const unsigned short*)d_in[2];
  p.wq = (const unsigned short*)d_in[3];
  p.wo = (const unsigned short*)d_in[4];
  p.wg = (const unsigned short*)d_in[6];
  p.wu = (const unsigned short*)d_in[7];
  p.wd = (const unsigned short*)d_in[8];
  p.out = (unsigned short*)d_out;
  p.ws = (unsigned char*)d_ws;
  (void)hipFuncSetAttribute((const void*)fused_decode_kernel, hipFuncAttributeMaxDynamicSharedMemorySize, LDS_BYTES);
  hipLaunchKernelGGL(fused_decode_kernel, dim3(NWG), dim3(NTHR), LDS_BYTES, stream, p);
}
```

```cpp
#include <hip/hip_runtime.h>
#include <hip/hip_bf16.h>
#include <stdint.h>

typedef unsigned v4u __attribute__((ext_vector_type(4)));
typedef unsigned v2u __attribute__((ext_vector_type(2)));
typedef float f32x4 __attribute__((ext_vector_type(4)));
typedef float f32x2 __attribute__((ext_vector_type(2)));
typedef short bf16x8 __attribute__((ext_vector_type(8)));
typedef short s16x4 __attribute__((ext_vector_type(4)));
typedef unsigned long long u64;
#define LAS __attribute__((address_space(3)))

constexpr int DM = 512, HQ = 32, HKV = 8, SK = 32768, DH = 128, WIN = 4096, FF = 1792;
constexpr int NSPLIT = 16, KPW = WIN / NSPLIT;
constexpr int NWG = 256, NTHR = 512;
constexpr unsigned SPIN_CAP = 1u << 18;
#define DSTART 2
#define D3A 1
#define D3B 3
#define D4A 24
#define D4B 1

constexpr int WS_Q = 4096;
constexpr int WS_G = WS_Q + 2 * 4 * 2048 * 16;
constexpr int WS_ML = WS_G + 16 * 896 * 16;
constexpr int WS_PO = WS_ML + 32768;
constexpr int WS_F = WS_PO + 1048576;
constexpr int WS_END = WS_F + 1048576;

constexpr int LK = 0;
constexpr int LV = 65536;
constexpr int LGU = LV + 49152;
constexpr int LW = 131072;
constexpr int LD = 147456;
constexpr int LS = 155648;
constexpr int S_XN = LS;
constexpr int XN_ST = 1040;
constexpr int S_Q = S_XN + 2 * XN_ST;
constexpr int Q_ST = 272;
constexpr int S_P = S_Q + 4 * Q_ST;
constexpr int P_ST = 528;
constexpr int S_MX = S_P + 4 * P_ST;
constexpr int S_LS = S_MX + 64;
constexpr int S_O = S_LS + 64;
constexpr int O_ST = 144;
constexpr int S_H = S_O + 2 * O_ST;
constexpr int H_ST = 80;
constexpr int LDS_BYTES = S_H + 2 * H_ST;

struct Params {
  const unsigned short* x;
  const unsigned short* kc;
  const unsigned short* vc;
  const unsigned short* wq;
  const unsigned short* wo;
  const unsigned short* wg;
  const unsigned short* wu;
  const unsigned short* wd;
  unsigned short* out;
  unsigned char* ws;
};

__device__ __forceinline__ float bflo(unsigned u) { return __builtin_bit_cast(float, u << 16); }
__device__ __forceinline__ float bfhi(unsigned u) { return __builtin_bit_cast(float, u & 0xffff0000u); }
typedef __bf16 bf16x2 __attribute__((ext_vector_type(2)));
__device__ __forceinline__ unsigned cvtpk(float lo, float hi) {
  return __builtin_bit_cast(unsigned, __builtin_convertvector((f32x2){lo, hi}, bf16x2));
}
__device__ __forceinline__ float bfr(float x) { return bflo(cvtpk(x, x)); }
template <int CTRL> __device__ __forceinline__ float dpp(float x) {
  return __builtin_bit_cast(float, __builtin_amdgcn_mov_dpp(__builtin_bit_cast(int, x), CTRL, 0xf, 0xf, true));
}
constexpr int XOR1 = 0xB1, XOR2 = 0x4E, XOR7 = 0x141, MIRR = 0x140, XOR8 = 0x128;
__device__ __forceinline__ float tr32(float a, float b) {
  auto s = __builtin_amdgcn_permlane32_swap(__float_as_uint(a), __float_as_uint(b), false, false);
  return __uint_as_float(s[0]) + __uint_as_float(s[1]);
}
__device__ __forceinline__ float tr16(float a, float b) {
  auto s = __builtin_amdgcn_permlane16_swap(__float_as_uint(a), __float_as_uint(b), false, false);
  return __uint_as_float(s[0]) + __uint_as_float(s[1]);
}
__device__ __forceinline__ float tr32m(float a, float b) {
  auto s = __builtin_amdgcn_permlane32_swap(__float_as_uint(a), __float_as_uint(b), false, false);
  return fmaxf(__uint_as_float(s[0]), __uint_as_float(s[1]));
}
__device__ __forceinline__ float tr16m(float a, float b) {
  auto s = __builtin_amdgcn_permlane16_swap(__float_as_uint(a), __float_as_uint(b), false, false);
  return fmaxf(__uint_as_float(s[0]), __uint_as_float(s[1]));
}
__device__ __forceinline__ float row16_sum(float x) {
  x += dpp<XOR1>(x); x += dpp<XOR2>(x); x += dpp<XOR7>(x); x += dpp<MIRR>(x);
  return x;
}
__device__ __forceinline__ float row16_max(float x) {
  x = fmaxf(x, dpp<XOR1>(x)); x = fmaxf(x, dpp<XOR2>(x)); x = fmaxf(x, dpp<XOR7>(x)); x = fmaxf(x, dpp<MIRR>(x));
  return x;
}
__device__ __forceinline__ float wave_sum(float x) {
  x = row16_sum(x);
  x = tr16(x, x);
  x = tr32(x, x);
  return x;
}
__device__ __forceinline__ v4u ld_sc1(__amdgpu_buffer_rsrc_t rs, int off) { return __builtin_amdgcn_raw_buffer_load_b128(rs, off, 0, 16); }
__device__ __forceinline__ void st_sc1(__amdgpu_buffer_rsrc_t rs, int off, v4u v) { __builtin_amdgcn_raw_buffer_store_b128(v, rs, off, 0, 16); }
__device__ __forceinline__ f32x4 mfma(v4u a, v4u b, f32x4 c) {
  return __builtin_amdgcn_mfma_f32_16x16x32_bf16(__builtin_bit_cast(bf16x8, a), __builtin_bit_cast(bf16x8, b), c, 0, 0, 0);
}
__device__ __forceinline__ v4u lds128(const LAS char* p) { return *reinterpret_cast<const LAS v4u*>(p); }
__device__ __forceinline__ v4u ldtr(const LAS char* a0, const LAS char* a1) {
  const s16x4 lo = __builtin_amdgcn_ds_read_tr16_b64_v4i16((LAS s16x4*)a0);
  const s16x4 hi = __builtin_amdgcn_ds_read_tr16_b64_v4i16((LAS s16x4*)a1);
  const v2u l2 = __builtin_bit_cast(v2u, lo), h2 = __builtin_bit_cast(v2u, hi);
  return v4u{l2.x, l2.y, h2.x, h2.y};
}
__device__ __forceinline__ int off256(int row, int ch) { return 256 * row + 16 * (ch ^ (((row & 3) << 2) | ((row >> 2) & 3))); }
__device__ __forceinline__ int offk(int row, int ch) { return 256 * row + 16 * (ch ^ (row & 15)); }
__device__ __forceinline__ int off128(int row, int ch) { return 128 * row + 16 * (ch ^ ((row & 2) | ((row >> 1) & 4))); }
__device__ __forceinline__ void dma1(const void* g, unsigned lds_dst) {
  unsigned keep;
  asm volatile("s_mov_b32 %0, m0\n\ts_mov_b32 m0, %2\n\ts_nop 0\n\tglobal_load_lds_dwordx4 %1, off nt\n\ts_mov_b32 m0, %0"
               : "=&s"(keep) : "v"(g), "s"(lds_dst) : "memory");
}
__device__ __forceinline__ void dma4(const void* sbase, unsigned v0, unsigned v1, unsigned v2, unsigned v3, unsigned lds_dst) {
  unsigned keep;
  asm volatile("s_mov_b32 %0, m0\n\ts_mov_b32 m0, %6\n\ts_nop 0\n\t"
               "global_load_lds_dwordx4 %1, %5 nt\n\t"
               "global_load_lds_dwordx4 %2, %5 offset:1024 nt\n\t"
               "global_load_lds_dwordx4 %3, %5 offset:2048 nt\n\t"
               "global_load_lds_dwordx4 %4, %5 offset:3072 nt\n\t"
               "s_mov_b32 m0, %0"
               : "=&s"(keep) : "v"(v0), "v"(v1), "v"(v2), "v"(v3), "s"(sbase), "s"(lds_dst) : "memory");
}
#define STAMP(k) do {} while (0)
#define STAMP_DECL do {} while (0)
#define STAMP_FLUSH do {} while (0)
#define MEM_FENCE() asm volatile("" ::: "memory")
#define BAR() asm volatile("s_waitcnt lgkmcnt(0)\n\ts_barrier" ::: "memory")
#define VMWAIT(n) asm volatile("s_waitcnt vmcnt(" #n ")" ::: "memory")
#define TAGOK(g) ((g).x == tA && (g).z == tB)

constexpr float LOG2E = 1.4426950408889634f;
constexpr float SCALE = 0.08838834764831845f;

__device__ __forceinline__ void proj_tile(const LAS char* sm, int tile, int kbase, int c, int lane, __amdgpu_buffer_rsrc_t rs,
                                          int dst, int bstride, unsigned tA, unsigned tB) {
  const int g = lane >> 4, q = (lane & 15) >> 2, pp = lane & 3;
  const LAS char* ap = sm + S_XN + (lane & 1) * XN_ST + (kbase + 8 * g) * 2;
  f32x4 acc = {0.f, 0.f, 0.f, 0.f};
#pragma unroll
  for (int ks = 0; ks < 4; ++ks) {
    const int r0 = 32 * ks + 8 * g + q;
    const v4u b = ldtr(sm + tile + off128(r0, 2 * c + (pp >> 1)) + 8 * (pp & 1), sm + tile + off128(r0 + 4, 2 * c + (pp >> 1)) + 8 * (pp & 1));
    acc = mfma(lds128(ap + ks * 64), b, acc);
  }
  const float n0 = dpp<XOR1>(acc[0]), n1 = dpp<XOR1>(acc[1]);
  const bool odd = lane & 1;
  const v4u gr = {tA, __float_as_uint(odd ? n1 : acc[0]), tB, __float_as_uint(odd ? acc[1] : n0)};
  if (lane < 16) st_sc1(rs, dst + (odd ? bstride : 0) + (c * 8 + (lane >> 1)) * 16, gr);
}

__global__ void __launch_bounds__(NTHR, 2) fused_decode_kernel(Params p) {
  extern __shared__ __attribute__((aligned(16))) char smem_[];
  const LAS char* smc = (const LAS char*)smem_;
  LAS char* sm = (LAS char*)smem_;
  const unsigned lb = (unsigned)(size_t)sm;
  const int tid = threadIdx.x, lane = tid & 63, wave = __builtin_amdgcn_readfirstlane(tid >> 6);
  const int bid = blockIdx.x, xcd = bid & 7, idx = bid >> 3;
  const __amdgpu_buffer_rsrc_t rs = __builtin_amdgcn_make_buffer_rsrc(p.ws, 0, WS_END, 0x00020000);
  STAMP_DECL;
  STAMP(0);
  u64 epoch;
  unsigned tA, tB;
#define EPOCH_GET() do { asm volatile("s_load_dwordx2 %0, %1, 0x0\n\ts_waitcnt lgkmcnt(0)" : "=s"(epoch) : "s"(p.ws) : "memory"); \
    const u64 tag64_ = (epoch + 1ull) * 0x9E3779B97F4A7C15ull; tA = (unsigned)tag64_; tB = (unsigned)(tag64_ >> 32); } while (0)

  const int qks = idx >> 3, qcol = xcd * 512 + (idx & 7) * 64;
  const int gtile = bid < 224 ? bid : 0, gmat = gtile / 112, gct = (gtile % 112) >> 2, gks = gtile & 3;
  const int ab = idx >> 4, asplit = idx & 15, kvh = xcd;
  const int rgi = xcd * 8 + (idx >> 2), cq = idx & 3, hp = (rgi >> 2) & 1, dq = rgi & 3;
  const int g4 = lane >> 4, q4 = (lane & 15) >> 2, p4 = lane & 3;

  if (wave >= 4) {
    const int lw = wave - 4;
    unsigned vo[4], ko[4];
#pragma unroll
    for (int j = 0; j < 4; ++j) { vo[j] = g4 * 256 + 16 * ((lane & 15) ^ ((g4 << 2) | j)); ko[j] = g4 * 256 + 16 * ((lane & 15) ^ (4 * j + g4)); }
    const size_t kvbase = ((size_t)(ab * HKV + kvh) * SK + (SK - WIN) + asplit * KPW) * DH;
    const char* kb = (const char*)(p.kc + kvbase) + lw * 4096;
    const char* vb = (const char*)(p.vc + kvbase) + lw * 4096;
#define KDMA(qd) dma4(kb + (qd) * 16384, ko[0], ko[1], ko[2], ko[3], lb + LK + (qd) * 16384 + lw * 4096)
#define VDMA(qd) dma4(vb + (qd) * 16384, vo[0], vo[1], vo[2], vo[3], lb + LV + (qd) * 16384 + lw * 4096)
    {
      const int r = 32 * lw + (lane >> 3);
#pragma unroll
      for (int j = 0; j < 4; ++j) {
        const int row = r + 8 * j, ch = (lane & 7) ^ ((row & 2) | ((row >> 1) & 4));
        dma1(p.wq + (size_t)(qks * 128 + row) * (HQ * DH) + qcol + ch * 8, lb + LW + (32 * lw + 8 * j) * 128);
      }
      __builtin_amdgcn_s_sleep(DSTART);
      KDMA(0);
      const unsigned short* wgu = gmat ? p.wu : p.wg;
      if (bid < 224) {
#pragma unroll
        for (int j = 0; j < 4; ++j) {
          const int row = r + 8 * j, ch = (lane & 7) ^ ((row & 2) | ((row >> 1) & 4));
          dma1(wgu + (size_t)(gks * 128 + row) * FF + gct * 64 + ch * 8, lb + LGU + (32 * lw + 8 * j) * 128);
        }
      }
      EPOCH_GET();
      if (bid < 224) VMWAIT(8); else VMWAIT(4);
    }
    STAMP(1);
    BAR();
    BAR();
    KDMA(1); KDMA(2);
    VMWAIT(4);
    STAMP(2);
    BAR();
    if (bid < 224) proj_tile(smc, LGU, gks * 128, lw, lane, rs, WS_G + ((gmat * 4 + gks) * 2 * 896 + gct * 32) * 16, 896 * 16, tA, tB);
    KDMA(3);
    VDMA(0); VDMA(1); VDMA(2);
    VMWAIT(12);
    STAMP(3);
    BAR();
    VDMA(3);
    BAR();
    VMWAIT(4);
    BAR();
    VMWAIT(0);
    STAMP(5);
    BAR();
    BAR();
    {
#pragma unroll
      for (int j = 0; j < 4; ++j) {
        const int k = 16 * lw + 4 * j + g4, grow = (kvh * 4 + 2 * hp + (k >> 5)) * DH + 32 * dq + (k & 31);
        dma1((const char*)(p.wo + (size_t)grow * DM + 128 * cq) + (vo[j] & 255), lb + LW + (16 * lw + 4 * j) * 256);
      }
#pragma unroll
      for (int j = 0; j < 2; ++j) {
        const int k = 8 * lw + 4 * j + g4, grow = 28 * rgi + (k < 28 ? k : 27);
        dma1((const char*)(p.wd + (size_t)grow * DM + 128 * cq) + (vo[2 * (lw & 1) + j] & 255), lb + LD + (8 * lw + 4 * j) * 256);
      }
    }
    VMWAIT(0);
    BAR();
    v4u bw[2][2];
    f32x4 fb[2];
    {
      const int r0 = 8 * g4 + q4, e = 8 * (p4 & 1);
      const v4u ha = lds128(smc + S_H + (lane & 1) * H_ST + g4 * 16);
#pragma unroll
      for (int t = 0; t < 2; ++t) {
        const int c0 = 4 * lw + 2 * t + (p4 >> 1);
        bw[t][0] = ldtr(smc + LW + off256(r0, c0) + e, smc + LW + off256(r0 + 4, c0) + e);
        bw[t][1] = ldtr(smc + LW + off256(r0 + 32, c0) + e, smc + LW + off256(r0 + 36, c0) + e);
        fb[t] = mfma(ha, ldtr(smc + LD + off256(r0, c0) + e, smc + LD + off256(r0 + 4, c0) + e), f32x4{0.f, 0.f, 0.f, 0.f});
      }
    }
    const int fo = WS_F + (rgi * 1024 + 128 * cq + 32 * lw + lane) * 16;
    const LAS char* oa = smc + S_O + (lane & 1) * O_ST + g4 * 16;
    STAMP(7);
    BAR();
    {
      const v4u o0 = lds128(oa), o1 = lds128(oa + 64);
#pragma unroll
      for (int t = 0; t < 2; ++t) {
        f32x4 fa = mfma(o0, bw[t][0], f32x4{0.f, 0.f, 0.f, 0.f});
        fa = mfma(o1, bw[t][1], fa);
        if (lane < 16) {
          st_sc1(rs, fo + t * 256, v4u{tA, __float_as_uint(fa[0]), tB, __float_as_uint(fb[t][0])});
          st_sc1(rs, fo + t * 256 + 512 * 16, v4u{tA, __float_as_uint(fa[1]), tB, __float_as_uint(fb[t][1])});
        }
      }
    }
    STAMP(8);
  } else {
    const v4u xa = *reinterpret_cast<const v4u*>(p.x + (wave & 1) * DM + 8 * lane);
    EPOCH_GET();
    if (wave < 2) {
      float e[8];
#pragma unroll
      for (int k = 0; k < 4; ++k) { e[2 * k] = bflo(xa[k]); e[2 * k + 1] = bfhi(xa[k]); }
      float s = ((e[0] + e[1]) + (e[2] + e[3])) + ((e[4] + e[5]) + (e[6] + e[7]));
      s = wave_sum(s) * (1.f / DM);
      float v = 0.f;
#pragma unroll
      for (int k = 0; k < 8; ++k) { const float d = e[k] - s; v = fmaf(d, d, v); }
      v = wave_sum(v) * (1.f / DM);
      const float mean = bfr(s), den = bfr(sqrtf(bfr(bfr(v) + 1e-5f)));
      const float rc = __builtin_amdgcn_rcpf(den);
      v4u o;
#pragma unroll
      for (int k = 0; k < 4; ++k) {
        const unsigned dk = cvtpk(e[2 * k] - mean, e[2 * k + 1] - mean);
        const float d0 = bflo(dk), d1 = bfhi(dk);
        float t0 = d0 * rc, t1 = d1 * rc;
        t0 = fmaf(fmaf(-t0, den, d0), rc, t0); t1 = fmaf(fmaf(-t1, den, d1), rc, t1);
        o[k] = cvtpk(t0, t1);
      }
      *reinterpret_cast<LAS v4u*>(sm + S_XN + wave * XN_ST + lane * 16) = o;
    } else if (wave == 2) {
      if (lane < 2) *reinterpret_cast<LAS v2u*>(sm + S_H + lane * H_ST + 56) = v2u{0u, 0u};
    }
    STAMP(1);
    BAR();
    proj_tile(smc, LW, qks * 128, wave, lane, rs, WS_Q + (qks * 2048 + (qcol >> 1)) * 16, 4 * 2048 * 16, tA, tB);
    BAR();
    BAR();
    {
      const int qoff = WS_Q + (ab * 4 * 2048 + kvh * 256 + tid) * 16;
      v4u g[4];
#pragma unroll
      for (int k = 0; k < 4; ++k) g[k] = ld_sc1(rs, qoff + k * 2048 * 16);
      if (!__all(TAGOK(g[0]) && TAGOK(g[1]) && TAGOK(g[2]) && TAGOK(g[3]))) {
#pragma nounroll
        for (unsigned spins = 0; spins < SPIN_CAP; ++spins) {
          MEM_FENCE();
#pragma unroll
          for (int k = 0; k < 4; ++k)
            if (!TAGOK(g[k])) g[k] = ld_sc1(rs, qoff + k * 2048 * 16);
          if (__all(TAGOK(g[0]) && TAGOK(g[1]) && TAGOK(g[2]) && TAGOK(g[3]))) break;
        }
      }
      const float s0 = (__uint_as_float(g[0].y) + __uint_as_float(g[1].y)) + (__uint_as_float(g[2].y) + __uint_as_float(g[3].y));
      const float s1 = (__uint_as_float(g[0].w) + __uint_as_float(g[1].w)) + (__uint_as_float(g[2].w) + __uint_as_float(g[3].w));
      *reinterpret_cast<LAS unsigned*>(sm + S_Q + wave * Q_ST + lane * 4) = cvtpk(s0, s1);
    }
    STAMP(2);
    const int gi = tid >> 3, gb = gi >= 14, gcp = gi - 14 * gb;
    const int goff = WS_G + (((((tid >> 2) & 1) * 4 + (tid & 3)) * 2 + gb) * 896 + 14 * rgi + gcp) * 16;
    const bool gact = tid < 224;
    v4u gg = v4u{tA, 0u, tB, 0u};
    if (gact) gg = ld_sc1(rs, goff);
    BAR();
    STAMP(3);
    float sc[4];
    {
      v4u qf[4];
#pragma unroll
      for (int s = 0; s < 4; ++s) qf[s] = lds128(smc + S_Q + (lane & 3) * Q_ST + g4 * 16 + s * 64);
      f32x4 at[4];
#pragma unroll
      for (int T = 0; T < 4; ++T) {
        at[T] = f32x4{0.f, 0.f, 0.f, 0.f};
        const int key = 64 * wave + 16 * T + (lane & 15);
#pragma unroll
        for (int s = 0; s < 4; ++s) at[T] = mfma(qf[s], lds128(smc + LK + offk(key, 4 * s + g4)), at[T]);
      }
      const f32x4 alo = (lane & 16) ? at[1] : at[0], ahi = (lane & 16) ? at[3] : at[2];
      const f32x4 acc = (lane & 32) ? ahi : alo;
      const unsigned u0 = cvtpk(acc[0], acc[1]), u1 = cvtpk(acc[2], acc[3]);
      const unsigned w0 = cvtpk(bflo(u0) * SCALE, bfhi(u0) * SCALE), w1 = cvtpk(bflo(u1) * SCALE, bfhi(u1) * SCALE);
      sc[0] = bflo(w0); sc[1] = bfhi(w0); sc[2] = bflo(w1); sc[3] = bfhi(w1);
      const float m = row16_max(tr16m(tr32m(sc[0], sc[2]), tr32m(sc[1], sc[3])));
      if ((lane & 15) == 0) *reinterpret_cast<LAS float*>(sm + S_MX + wave * 16 + g4 * 4) = m;
    }
    STAMP(11);
    BAR();
    {
      const LAS f32x4* mx = reinterpret_cast<const LAS f32x4*>(smc + S_MX);
      const f32x4 m0 = mx[0], m1 = mx[1], m2 = mx[2], m3 = mx[3];
      float pr[4];
#pragma unroll
      for (int h = 0; h < 4; ++h) {
        const float M = fmaxf(fmaxf(m0[h], m1[h]), fmaxf(m2[h], m3[h]));
        pr[h] = __builtin_amdgcn_exp2f((sc[h] - M) * LOG2E);
      }
      const unsigned u0 = cvtpk(pr[0], pr[1]), u1 = cvtpk(pr[2], pr[3]);
      LAS unsigned short* pp = reinterpret_cast<LAS unsigned short*>(sm + S_P) + 64 * wave + lane;
      pp[0] = (unsigned short)u0; pp[P_ST / 2] = (unsigned short)(u0 >> 16); pp[P_ST] = (unsigned short)u1; pp[3 * P_ST / 2] = (unsigned short)(u1 >> 16);
      const float l = row16_sum(tr16(tr32(bflo(u0), bflo(u1)), tr32(bfhi(u0), bfhi(u1))));
      if ((lane & 15) == 0) *reinterpret_cast<LAS float*>(sm + S_LS + wave * 16 + g4 * 4) = l;
    }
    {
      if (!__all(TAGOK(gg))) {
#pragma nounroll
        for (unsigned spins = 0; spins < SPIN_CAP; ++spins) {
          MEM_FENCE();
          if (!TAGOK(gg)) gg = ld_sc1(rs, goff);
          if (__all(TAGOK(gg))) break;
        }
      }
      float s0 = __uint_as_float(gg.y), s1 = __uint_as_float(gg.w);
      s0 += dpp<XOR1>(s0); s0 += dpp<XOR2>(s0); s1 += dpp<XOR1>(s1); s1 += dpp<XOR2>(s1);
      const unsigned own = cvtpk(s0, s1), oth = cvtpk(dpp<XOR7>(s0), dpp<XOR7>(s1));
      const float g0 = bflo(own), g1 = bfhi(own);
      const unsigned sg = cvtpk(g0 * __builtin_amdgcn_rcpf(1.f + __expf(-g0)), g1 * __builtin_amdgcn_rcpf(1.f + __expf(-g1)));
      const unsigned hh = cvtpk(bflo(sg) * bflo(oth), bfhi(sg) * bfhi(oth));
      if (gact && (lane & 7) == 0) *reinterpret_cast<LAS unsigned*>(sm + S_H + gb * H_ST + gcp * 4) = hh;
    }
    STAMP(4);
    BAR();
    if (wave == 3 && lane < 8) {
      const int h = lane & 3;
      const LAS float* mx = reinterpret_cast<const LAS float*>(smc + S_MX);
      const LAS float* ls = reinterpret_cast<const LAS float*>(smc + S_LS);
      const float M = fmaxf(fmaxf(mx[h], mx[4 + h]), fmaxf(mx[8 + h], mx[12 + h]));
      const float L = (ls[h] + ls[4 + h]) + (ls[8 + h] + ls[12 + h]);
      st_sc1(rs, WS_ML + (((ab * HKV + kvh) * NSPLIT + asplit) * 8 + lane) * 16, v4u{tA, __float_as_uint(M), tB, __float_as_uint(L)});
    }
    {
      f32x4 a0 = {0.f, 0.f, 0.f, 0.f}, a1 = {0.f, 0.f, 0.f, 0.f};
      const LAS char* pa = smc + S_P + (lane & 3) * P_ST + g4 * 16;
#pragma unroll
      for (int ks = 0; ks < 8; ++ks) {
        if (ks == 6) { BAR(); STAMP(5); }
        const v4u a = lds128(pa + ks * 64);
        const int r0 = 32 * ks + 8 * g4 + q4, c0 = 4 * wave + (p4 >> 1), e = 8 * (p4 & 1);
        a0 = mfma(a, ldtr(smc + LV + off256(r0, c0) + e, smc + LV + off256(r0 + 4, c0) + e), a0);
        a1 = mfma(a, ldtr(smc + LV + off256(r0, c0 + 2) + e, smc + LV + off256(r0 + 4, c0 + 2) + e), a1);
      }
      asm volatile("" :: "v"(a0), "v"(a1));
      STAMP(10);
      if (lane < 16) {
        const int po = WS_PO + ((((ab * HKV + kvh) * 2) * NSPLIT + asplit) * 128 + 32 * wave + lane) * 16;
        st_sc1(rs, po, v4u{tA, __float_as_uint(a0[0]), tB, __float_as_uint(a0[1])});
        st_sc1(rs, po + 256, v4u{tA, __float_as_uint(a1[0]), tB, __float_as_uint(a1[1])});
        st_sc1(rs, po + NSPLIT * 128 * 16, v4u{tA, __float_as_uint(a0[2]), tB, __float_as_uint(a0[3])});
        st_sc1(rs, po + NSPLIT * 128 * 16 + 256, v4u{tA, __float_as_uint(a1[2]), tB, __float_as_uint(a1[3])});
      }
    }
    BAR();
    STAMP(6);
    {
      const int cb = wave >> 1, cs = lane & 15, d0 = (wave & 1) * 16 + g4;
      const int ooff = WS_PO + ((((cb * HKV + kvh) * 2 + hp) * NSPLIT + cs) * 128 + 32 * dq + d0) * 16;
      const int moff = WS_ML + (((cb * HKV + kvh) * NSPLIT + cs) * 8 + 2 * hp) * 16;
      LAS unsigned short* od = reinterpret_cast<LAS unsigned short*>(sm + S_O) + cb * (O_ST / 2) + ((lane >> 1) & 1) * 32 + d0 + 4 * ((lane >> 2) & 3);
      BAR();
      __builtin_amdgcn_s_sleep(D3A);
      v4u go[4], gm0, gm1;
      gm0 = ld_sc1(rs, moff); gm1 = ld_sc1(rs, moff + 16);
#pragma unroll
      for (int j = 0; j < 4; ++j) go[j] = ld_sc1(rs, ooff + 64 * j);
      if (!__all(TAGOK(gm0) && TAGOK(gm1))) {
#pragma nounroll
        for (unsigned spins = 0; spins < SPIN_CAP; ++spins) {
          MEM_FENCE();
          if (!TAGOK(gm0)) gm0 = ld_sc1(rs, moff);
          if (!TAGOK(gm1)) gm1 = ld_sc1(rs, moff + 16);
          if (__all(TAGOK(gm0) && TAGOK(gm1))) break;
        }
      }
      const float ma = __uint_as_float(gm0.y), mb = __uint_as_float(gm1.y);
      const float wa = __builtin_amdgcn_exp2f((ma - row16_max(ma)) * LOG2E), wb = __builtin_amdgcn_exp2f((mb - row16_max(mb)) * LOG2E);
      const float na = wa * __builtin_amdgcn_rcpf(row16_sum(wa * __uint_as_float(gm0.w)));
      const float nb = wb * __builtin_amdgcn_rcpf(row16_sum(wb * __uint_as_float(gm1.w)));
      asm volatile("" :: "v"(na), "v"(nb));
#define ALLOK (TAGOK(go[0]) && TAGOK(go[1]) && TAGOK(go[2]) && TAGOK(go[3]))
      if (!__all(ALLOK)) {
#pragma nounroll
        for (unsigned spins = 0; spins < SPIN_CAP; ++spins) {
          __builtin_amdgcn_s_sleep(D3B);
          MEM_FENCE();
#pragma unroll
          for (int j = 0; j < 4; ++j)
            if (!TAGOK(go[j])) go[j] = ld_sc1(rs, ooff + 64 * j);
          if (__all(ALLOK)) break;
        }
      }
#undef ALLOK
      STAMP(7);
      float v[4][2];
#pragma unroll
      for (int j = 0; j < 4; ++j) { v[j][0] = na * __uint_as_float(go[j].y); v[j][1] = nb * __uint_as_float(go[j].w); }
      float u[2][2], w2[2];
#pragma unroll
      for (int j = 0; j < 2; ++j)
#pragma unroll
        for (int h = 0; h < 2; ++h) { const float a = v[j][h] + dpp<XOR8>(v[j][h]), b = v[j + 2][h] + dpp<XOR8>(v[j + 2][h]); u[j][h] = (lane & 8) ? b : a; }
#pragma unroll
      for (int h = 0; h < 2; ++h) { const float a = u[0][h] + dpp<XOR7>(u[0][h]), b = u[1][h] + dpp<XOR7>(u[1][h]); w2[h] = (lane & 4) ? b : a; }
      const float a = w2[0] + dpp<XOR2>(w2[0]), b = w2[1] + dpp<XOR2>(w2[1]);
      float r = (lane & 2) ? b : a;
      r += dpp<XOR1>(r);
      if ((lane & 1) == 0) *od = (unsigned short)cvtpk(r, r);
    }
    BAR();
    STAMP(8);
  }

  if (wave == 0) {
    const int ob = (xcd * 32 + idx) * 4;
    const unsigned short xfin = p.x[ob + g4];
    v4u g[4];
    const int o0 = WS_F + (lane * 1024 + ob) * 16;
    __builtin_amdgcn_s_sleep(D4A);
#pragma unroll
    for (int k = 0; k < 4; ++k) g[k] = ld_sc1(rs, o0 + k * 16);
    if (!__all(TAGOK(g[0]) && TAGOK(g[1]) && TAGOK(g[2]) && TAGOK(g[3]))) {
#pragma nounroll
      for (unsigned spins = 0; spins < SPIN_CAP; ++spins) {
        __builtin_amdgcn_s_sleep(D4B);
        MEM_FENCE();
#pragma unroll
        for (int k = 0; k < 4; ++k)
          if (!TAGOK(g[k])) g[k] = ld_sc1(rs, o0 + k * 16);
        if (__all(TAGOK(g[0]) && TAGOK(g[1]) && TAGOK(g[2]) && TAGOK(g[3]))) break;
      }
    }
    STAMP(9);
    float va = tr16(tr32(__uint_as_float(g[0].y), __uint_as_float(g[2].y)), tr32(__uint_as_float(g[1].y), __uint_as_float(g[3].y)));
    float vb = tr16(tr32(__uint_as_float(g[0].w), __uint_as_float(g[2].w)), tr32(__uint_as_float(g[1].w), __uint_as_float(g[3].w)));
    va = row16_sum(va); vb = row16_sum(vb);
    if ((lane & 15) == 0) {
      const float r = bfr(bflo(xfin) + bfr(va)) + bfr(vb);
      p.out[ob + g4] = (unsigned short)cvtpk(r, r);
      if (bid == 0 && lane == 0) *reinterpret_cast<volatile u64*>(p.ws) = epoch + 1ull;
    }
  }
  STAMP_FLUSH;
}

extern "C" void kernel_launch(void* const* d_in, const int* in_sizes, int n_in, void* d_out, int out_size, void* d_ws,
                              size_t ws_size, hipStream_t stream) {
  Params p;
  p.x = (const unsigned short*)d_in[0];
  p.kc = (const unsigned short*)d_in[1];
  p.vc = (const unsigned short*)d_in[2];
  p.wq = (const unsigned short*)d_in[3];
  p.wo = (const unsigned short*)d_in[4];
  p.wg = (const unsigned short*)d_in[6];
  p.wu = (const unsigned short*)d_in[7];
  p.wd = (const unsigned short*)d_in[8];
  p.out = (unsigned short*)d_out;
  p.ws = (unsigned char*)d_ws;
  (void)hipFuncSetAttribute((const void*)fused_decode_kernel, hipFuncAttributeMaxDynamicSharedMemorySize, LDS_BYTES);
  hipLaunchKernelGGL(fused_decode_kernel, dim3(NWG), dim3(NTHR), LDS_BYTES, stream, p);
}
```

```cpp
#include <hip/hip_runtime.h>
#include <hip/hip_bf16.h>
#include <stdint.h>

typedef unsigned v4u __attribute__((ext_vector_type(4)));
typedef unsigned v2u __attribute__((ext_vector_type(2)));
typedef float f32x4 __attribute__((ext_vector_type(4)));
typedef float f32x2 __attribute__((ext_vector_type(2)));
typedef short bf16x8 __attribute__((ext_vector_type(8)));
typedef short s16x4 __attribute__((ext_vector_type(4)));
typedef unsigned long long u64;
#define LAS __attribute__((address_space(3)))

constexpr int DM = 512, HQ = 32, HKV = 8, SK = 32768, DH = 128, WIN = 4096, FF = 1792;
constexpr int NSPLIT = 16, KPW = WIN / NSPLIT;
constexpr int NWG = 256, NTHR = 512;
constexpr unsigned SPIN_CAP = 1u << 18;
constexpr int DSTART = 2, D3A = 1, D3B = 3, D4A = 24, D4B = 1;

constexpr int WS_Q = 4096;
constexpr int WS_G = WS_Q + 2 * 4 * 2048 * 16;
constexpr int WS_ML = WS_G + 16 * 896 * 16;
constexpr int WS_PO = WS_ML + 32768;
constexpr int WS_F = WS_PO + 1048576;
constexpr int WS_END = WS_F + 1048576;

constexpr int LK = 0;
constexpr int LV = 65536;
constexpr int LGU = LV + 49152;
constexpr int LW = 131072;
constexpr int LD = 147456;
constexpr int LS = 155648;
constexpr int S_XN = LS;
constexpr int XN_ST = 1040;
constexpr int S_Q = S_XN + 2 * XN_ST;
constexpr int Q_ST = 272;
constexpr int S_P = S_Q + 4 * Q_ST;
constexpr int P_ST = 528;
constexpr int S_MX = S_P + 4 * P_ST;
constexpr int S_LS = S_MX + 64;
constexpr int S_O = S_LS + 64;
constexpr int O_ST = 144;
constexpr int S_H = S_O + 2 * O_ST;
constexpr int H_ST = 80;
constexpr int LDS_BYTES = S_H + 2 * H_ST;

struct Params {
  const unsigned short* x;
  const unsigned short* kc;
  const unsigned short* vc;
  const unsigned short* wq;
  const unsigned short* wo;
  const unsigned short* wg;
  const unsigned short* wu;
  const unsigned short* wd;
  unsigned short* out;
  unsigned char* ws;
};

__device__ __forceinline__ float bflo(unsigned u) { return __builtin_bit_cast(float, u << 16); }
__device__ __forceinline__ float bfhi(unsigned u) { return __builtin_bit_cast(float, u & 0xffff0000u); }
typedef __bf16 bf16x2 __attribute__((ext_vector_type(2)));
__device__ __forceinline__ unsigned cvtpk(float lo, float hi) {
  return __builtin_bit_cast(unsigned, __builtin_convertvector((f32x2){lo, hi}, bf16x2));
}
__device__ __forceinline__ float bfr(float x) { return bflo(cvtpk(x, x)); }
template <int CTRL> __device__ __forceinline__ float dpp(float x) {
  return __builtin_bit_cast(float, __builtin_amdgcn_mov_dpp(__builtin_bit_cast(int, x), CTRL, 0xf, 0xf, true));
}
constexpr int XOR1 = 0xB1, XOR2 = 0x4E, XOR7 = 0x141, MIRR = 0x140, XOR8 = 0x128;
__device__ __forceinline__ float tr32(float a, float b) {
  auto s = __builtin_amdgcn_permlane32_swap(__float_as_uint(a), __float_as_uint(b), false, false);
  return __uint_as_float(s[0]) + __uint_as_float(s[1]);
}
__device__ __forceinline__ float tr16(float a, float b) {
  auto s = __builtin_amdgcn_permlane16_swap(__float_as_uint(a), __float_as_uint(b), false, false);
  return __uint_as_float(s[0]) + __uint_as_float(s[1]);
}
__device__ __forceinline__ float tr32m(float a, float b) {
  auto s = __builtin_amdgcn_permlane32_swap(__float_as_uint(a), __float_as_uint(b), false, false);
  return fmaxf(__uint_as_float(s[0]), __uint_as_float(s[1]));
}
__device__ __forceinline__ float tr16m(float a, float b) {
  auto s = __builtin_amdgcn_permlane16_swap(__float_as_uint(a), __float_as_uint(b), false, false);
  return fmaxf(__uint_as_float(s[0]), __uint_as_float(s[1]));
}
__device__ __forceinline__ float row16_sum(float x) {
  x += dpp<XOR1>(x); x += dpp<XOR2>(x); x += dpp<XOR7>(x); x += dpp<MIRR>(x);
  return x;
}
__device__ __forceinline__ float row16_max(float x) {
  x = fmaxf(x, dpp<XOR1>(x)); x = fmaxf(x, dpp<XOR2>(x)); x = fmaxf(x, dpp<XOR7>(x)); x = fmaxf(x, dpp<MIRR>(x));
  return x;
}
__device__ __forceinline__ float wave_sum(float x) {
  x = row16_sum(x);
  x = tr16(x, x);
  x = tr32(x, x);
  return x;
}
__device__ __forceinline__ v4u ld_sc1(__amdgpu_buffer_rsrc_t rs, int off) { return __builtin_amdgcn_raw_buffer_load_b128(rs, off, 0, 16); }
__device__ __forceinline__ void st_sc1(__amdgpu_buffer_rsrc_t rs, int off, v4u v) { __builtin_amdgcn_raw_buffer_store_b128(v, rs, off, 0, 16); }
__device__ __forceinline__ f32x4 mfma(v4u a, v4u b, f32x4 c) {
  return __builtin_amdgcn_mfma_f32_16x16x32_bf16(__builtin_bit_cast(bf16x8, a), __builtin_bit_cast(bf16x8, b), c, 0, 0, 0);
}
__device__ __forceinline__ v4u lds128(const LAS char* p) { return *reinterpret_cast<const LAS v4u*>(p); }
__device__ __forceinline__ v4u ldtr(const LAS char* a0, const LAS char* a1) {
  const s16x4 lo = __builtin_amdgcn_ds_read_tr16_b64_v4i16((LAS s16x4*)a0);
  const s16x4 hi = __builtin_amdgcn_ds_read_tr16_b64_v4i16((LAS s16x4*)a1);
  const v2u l2 = __builtin_bit_cast(v2u, lo), h2 = __builtin_bit_cast(v2u, hi);
  return v4u{l2.x, l2.y, h2.x, h2.y};
}
__device__ __forceinline__ int off256(int row, int ch) { return 256 * row + 16 * (ch ^ (((row & 3) << 2) | ((row >> 2) & 3))); }
__device__ __forceinline__ int offk(int row, int ch) { return 256 * row + 16 * (ch ^ (row & 15)); }
__device__ __forceinline__ int off128(int row, int ch) { return 128 * row + 16 * (ch ^ ((row & 2) | ((row >> 1) & 4))); }
__device__ __forceinline__ void dma1(const void* g, unsigned lds_dst) {
  unsigned keep;
  asm volatile("s_mov_b32 %0, m0\n\ts_mov_b32 m0, %2\n\ts_nop 0\n\tglobal_load_lds_dwordx4 %1, off nt\n\ts_mov_b32 m0, %0"
               : "=&s"(keep) : "v"(g), "s"(lds_dst) : "memory");
}
__device__ __forceinline__ void dma4(const void* sbase, unsigned v0, unsigned v1, unsigned v2, unsigned v3, unsigned lds_dst) {
  unsigned keep;
  asm volatile("s_mov_b32 %0, m0\n\ts_mov_b32 m0, %6\n\ts_nop 0\n\t"
               "global_load_lds_dwordx4 %1, %5 nt\n\t"
               "global_load_lds_dwordx4 %2, %5 offset:1024 nt\n\t"
               "global_load_lds_dwordx4 %3, %5 offset:2048 nt\n\t"
               "global_load_lds_dwordx4 %4, %5 offset:3072 nt\n\t"
               "s_mov_b32 m0, %0"
               : "=&s"(keep) : "v"(v0), "v"(v1), "v"(v2), "v"(v3), "s"(sbase), "s"(lds_dst) : "memory");
}
#define MEM_FENCE() asm volatile("" ::: "memory")
#define BAR() asm volatile("s_waitcnt lgkmcnt(0)\n\ts_barrier" ::: "memory")
#define VMWAIT(n) asm volatile("s_waitcnt vmcnt(" #n ")" ::: "memory")
#define TAGOK(g) ((g).x == tA && (g).z == tB)

constexpr float LOG2E = 1.4426950408889634f;
constexpr float SCALE = 0.08838834764831845f;

__device__ __forceinline__ void proj_tile(const LAS char* sm, int tile, int kbase, int c, int lane, __amdgpu_buffer_rsrc_t rs,
                                          int dst, int bstride, unsigned tA, unsigned tB) {
  const int g = lane >> 4, q = (lane & 15) >> 2, pp = lane & 3;
  const LAS char* ap = sm + S_XN + (lane & 1) * XN_ST + (kbase + 8 * g) * 2;
  f32x4 acc = {0.f, 0.f, 0.f, 0.f};
#pragma unroll
  for (int ks = 0; ks < 4; ++ks) {
    const int r0 = 32 * ks + 8 * g + q;
    const v4u b = ldtr(sm + tile + off128(r0, 2 * c + (pp >> 1)) + 8 * (pp & 1), sm + tile + off128(r0 + 4, 2 * c + (pp >> 1)) + 8 * (pp & 1));
    acc = mfma(lds128(ap + ks * 64), b, acc);
  }
  const float n0 = dpp<XOR1>(acc[0]), n1 = dpp<XOR1>(acc[1]);
  const bool odd = lane & 1;
  const v4u gr = {tA, __float_as_uint(odd ? n1 : acc[0]), tB, __float_as_uint(odd ? acc[1] : n0)};
  if (lane < 16) st_sc1(rs, dst + (odd ? bstride : 0) + (c * 8 + (lane >> 1)) * 16, gr);
}

__global__ void __launch_bounds__(NTHR, 2) fused_decode_kernel(Params p) {
  extern __shared__ __attribute__((aligned(16))) char smem_[];
  const LAS char* smc = (const LAS char*)smem_;
  LAS char* sm = (LAS char*)smem_;
  const unsigned lb = (unsigned)(size_t)sm;
  const int tid = threadIdx.x, lane = tid & 63, wave = __builtin_amdgcn_readfirstlane(tid >> 6);
  const int bid = blockIdx.x, xcd = bid & 7, idx = bid >> 3;
  const __amdgpu_buffer_rsrc_t rs = __builtin_amdgcn_make_buffer_rsrc(p.ws, 0, WS_END, 0x00020000);
  u64 epoch;
  unsigned tA, tB;
#define EPOCH_GET() do { asm volatile("s_load_dwordx2 %0, %1, 0x0\n\ts_waitcnt lgkmcnt(0)" : "=s"(epoch) : "s"(p.ws) : "memory"); \
    const u64 tag64_ = (epoch + 1ull) * 0x9E3779B97F4A7C15ull; tA = (unsigned)tag64_; tB = (unsigned)(tag64_ >> 32); } while (0)

  const int qks = idx >> 3, qcol = xcd * 512 + (idx & 7) * 64;
  const int gtile = bid < 224 ? bid : 0, gmat = gtile / 112, gct = (gtile % 112) >> 2, gks = gtile & 3;
  const int ab = idx >> 4, asplit = idx & 15, kvh = xcd;
  const int rgi = xcd * 8 + (idx >> 2), cq = idx & 3, hp = (rgi >> 2) & 1, dq = rgi & 3;
  const int g4 = lane >> 4, q4 = (lane & 15) >> 2, p4 = lane & 3;

  if (wave >= 4) {
    const int lw = wave - 4;
    unsigned vo[4], ko[4];
#pragma unroll
    for (int j = 0; j < 4; ++j) { vo[j] = g4 * 256 + 16 * ((lane & 15) ^ ((g4 << 2) | j)); ko[j] = g4 * 256 + 16 * ((lane & 15) ^ (4 * j + g4)); }
    const size_t kvbase = ((size_t)(ab * HKV + kvh) * SK + (SK - WIN) + asplit * KPW) * DH;
    const char* kb = (const char*)(p.kc + kvbase) + lw * 4096;
    const char* vb = (const char*)(p.vc + kvbase) + lw * 4096;
#define KDMA(qd) dma4(kb + (qd) * 16384, ko[0], ko[1], ko[2], ko[3], lb + LK + (qd) * 16384 + lw * 4096)
#define VDMA(qd) dma4(vb + (qd) * 16384, vo[0], vo[1], vo[2], vo[3], lb + LV + (qd) * 16384 + lw * 4096)
    {
      const int r = 32 * lw + (lane >> 3);
#pragma unroll
      for (int j = 0; j < 4; ++j) {
        const int row = r + 8 * j, ch = (lane & 7) ^ ((row & 2) | ((row >> 1) & 4));
        dma1(p.wq + (size_t)(qks * 128 + row) * (HQ * DH) + qcol + ch * 8, lb + LW + (32 * lw + 8 * j) * 128);
      }
      __builtin_amdgcn_s_sleep(DSTART);
      KDMA(0);
      const unsigned short* wgu = gmat ? p.wu : p.wg;
      if (bid < 224) {
#pragma unroll
        for (int j = 0; j < 4; ++j) {
          const int row = r + 8 * j, ch = (lane & 7) ^ ((row & 2) | ((row >> 1) & 4));
          dma1(wgu + (size_t)(gks * 128 + row) * FF + gct * 64 + ch * 8, lb + LGU + (32 * lw + 8 * j) * 128);
        }
      }
      EPOCH_GET();
      if (bid < 224) VMWAIT(8); else VMWAIT(4);
    }
    BAR();
    BAR();
    KDMA(1); KDMA(2);
    VMWAIT(4);
    BAR();
    if (bid < 224) proj_tile(smc, LGU, gks * 128, lw, lane, rs, WS_G + ((gmat * 4 + gks) * 2 * 896 + gct * 32) * 16, 896 * 16, tA, tB);
    KDMA(3);
    VDMA(0); VDMA(1); VDMA(2);
    VMWAIT(12);
    BAR();
    VDMA(3);
    BAR();
    VMWAIT(4);
    BAR();
    VMWAIT(0);
    BAR();
    BAR();
    {
#pragma unroll
      for (int j = 0; j < 4; ++j) {
        const int k = 16 * lw + 4 * j + g4, grow = (kvh * 4 + 2 * hp + (k >> 5)) * DH + 32 * dq + (k & 31);
        dma1((const char*)(p.wo + (size_t)grow * DM + 128 * cq) + (vo[j] & 255), lb + LW + (16 * lw + 4 * j) * 256);
      }
#pragma unroll
      for (int j = 0; j < 2; ++j) {
        const int k = 8 * lw + 4 * j + g4, grow = 28 * rgi + (k < 28 ? k : 27);
        dma1((const char*)(p.wd + (size_t)grow * DM + 128 * cq) + (vo[2 * (lw & 1) + j] & 255), lb + LD + (8 * lw + 4 * j) * 256);
      }
    }
    VMWAIT(0);
    BAR();
    v4u bw[2][2];
    f32x4 fb[2];
    {
      const int r0 = 8 * g4 + q4, e = 8 * (p4 & 1);
      const v4u ha = lds128(smc + S_H + (lane & 1) * H_ST + g4 * 16);
#pragma unroll
      for (int t = 0; t < 2; ++t) {
        const int c0 = 4 * lw + 2 * t + (p4 >> 1);
        bw[t][0] = ldtr(smc + LW + off256(r0, c0) + e, smc + LW + off256(r0 + 4, c0) + e);
        bw[t][1] = ldtr(smc + LW + off256(r0 + 32, c0) + e, smc + LW + off256(r0 + 36, c0) + e);
        fb[t] = mfma(ha, ldtr(smc + LD + off256(r0, c0) + e, smc + LD + off256(r0 + 4, c0) + e), f32x4{0.f, 0.f, 0.f, 0.f});
      }
    }
    const int fo = WS_F + (rgi * 1024 + 128 * cq + 32 * lw + lane) * 16;
    const LAS char* oa = smc + S_O + (lane & 1) * O_ST + g4 * 16;
    BAR();
    {
      const v4u o0 = lds128(oa), o1 = lds128(oa + 64);
#pragma unroll
      for (int t = 0; t < 2; ++t) {
        f32x4 fa = mfma(o0, bw[t][0], f32x4{0.f, 0.f, 0.f, 0.f});
        fa = mfma(o1, bw[t][1], fa);
        if (lane < 16) {
          st_sc1(rs, fo + t * 256, v4u{tA, __float_as_uint(fa[0]), tB, __float_as_uint(fb[t][0])});
          st_sc1(rs, fo + t * 256 + 512 * 16, v4u{tA, __float_as_uint(fa[1]), tB, __float_as_uint(fb[t][1])});
        }
      }
    }
  } else {
    const v4u xa = *reinterpret_cast<const v4u*>(p.x + (wave & 1) * DM + 8 * lane);
    EPOCH_GET();
    if (wave < 2) {
      float e[8];
#pragma unroll
      for (int k = 0; k < 4; ++k) { e[2 * k] = bflo(xa[k]); e[2 * k + 1] = bfhi(xa[k]); }
      float s = ((e[0] + e[1]) + (e[2] + e[3])) + ((e[4] + e[5]) + (e[6] + e[7]));
      s = wave_sum(s) * (1.f / DM);
      float v = 0.f;
#pragma unroll
      for (int k = 0; k < 8; ++k) { const float d = e[k] - s; v = fmaf(d, d, v); }
      v = wave_sum(v) * (1.f / DM);
      const float mean = bfr(s), den = bfr(sqrtf(bfr(bfr(v) + 1e-5f)));
      const float rc = __builtin_amdgcn_rcpf(den);
      v4u o;
#pragma unroll
      for (int k = 0; k < 4; ++k) {
        const unsigned dk = cvtpk(e[2 * k] - mean, e[2 * k + 1] - mean);
        const float d0 = bflo(dk), d1 = bfhi(dk);
        float t0 = d0 * rc, t1 = d1 * rc;
        t0 = fmaf(fmaf(-t0, den, d0), rc, t0); t1 = fmaf(fmaf(-t1, den, d1), rc, t1);
        o[k] = cvtpk(t0, t1);
      }
      *reinterpret_cast<LAS v4u*>(sm + S_XN + wave * XN_ST + lane * 16) = o;
    } else if (wave == 2) {
      if (lane < 2) *reinterpret_cast<LAS v2u*>(sm + S_H + lane * H_ST + 56) = v2u{0u, 0u};
    }
    BAR();
    proj_tile(smc, LW, qks * 128, wave, lane, rs, WS_Q + (qks * 2048 + (qcol >> 1)) * 16, 4 * 2048 * 16, tA, tB);
    BAR();
    BAR();
    {
      const int qoff = WS_Q + (ab * 4 * 2048 + kvh * 256 + tid) * 16;
      v4u g[4];
#pragma unroll
      for (int k = 0; k < 4; ++k) g[k] = ld_sc1(rs, qoff + k * 2048 * 16);
      if (!__all(TAGOK(g[0]) && TAGOK(g[1]) && TAGOK(g[2]) && TAGOK(g[3]))) {
#pragma nounroll
        for (unsigned spins = 0; spins < SPIN_CAP; ++spins) {
          MEM_FENCE();
#pragma unroll
          for (int k = 0; k < 4; ++k)
            if (!TAGOK(g[k])) g[k] = ld_sc1(rs, qoff + k * 2048 * 16);
          if (__all(TAGOK(g[0]) && TAGOK(g[1]) && TAGOK(g[2]) && TAGOK(g[3]))) break;
        }
      }
      const float s0 = (__uint_as_float(g[0].y) + __uint_as_float(g[1].y)) + (__uint_as_float(g[2].y) + __uint_as_float(g[3].y));
      const float s1 = (__uint_as_float(g[0].w) + __uint_as_float(g[1].w)) + (__uint_as_float(g[2].w) + __uint_as_float(g[3].w));
      *reinterpret_cast<LAS unsigned*>(sm + S_Q + wave * Q_ST + lane * 4) = cvtpk(s0, s1);
    }
    const int gi = tid >> 3, gb = gi >= 14, gcp = gi - 14 * gb;
    const int goff = WS_G + (((((tid >> 2) & 1) * 4 + (tid & 3)) * 2 + gb) * 896 + 14 * rgi + gcp) * 16;
    const bool gact = tid < 224;
    v4u gg = v4u{tA, 0u, tB, 0u};
    if (gact) gg = ld_sc1(rs, goff);
    BAR();
    float sc[4];
    {
      v4u qf[4];
#pragma unroll
      for (int s = 0; s < 4; ++s) qf[s] = lds128(smc + S_Q + (lane & 3) * Q_ST + g4 * 16 + s * 64);
      f32x4 at[4];
#pragma unroll
      for (int T = 0; T < 4; ++T) {
        at[T] = f32x4{0.f, 0.f, 0.f, 0.f};
        const int key = 64 * wave + 16 * T + (lane & 15);
#pragma unroll
        for (int s = 0; s < 4; ++s) at[T] = mfma(qf[s], lds128(smc + LK + offk(key, 4 * s + g4)), at[T]);
      }
      const f32x4 alo = (lane & 16) ? at[1] : at[0], ahi = (lane & 16) ? at[3] : at[2];
      const f32x4 acc = (lane & 32) ? ahi : alo;
      const unsigned u0 = cvtpk(acc[0], acc[1]), u1 = cvtpk(acc[2], acc[3]);
      const unsigned w0 = cvtpk(bflo(u0) * SCALE, bfhi(u0) * SCALE), w1 = cvtpk(bflo(u1) * SCALE, bfhi(u1) * SCALE);
      sc[0] = bflo(w0); sc[1] = bfhi(w0); sc[2] = bflo(w1); sc[3] = bfhi(w1);
      const float m = row16_max(tr16m(tr32m(sc[0], sc[2]), tr32m(sc[1], sc[3])));
      if ((lane & 15) == 0) *reinterpret_cast<LAS float*>(sm + S_MX + wave * 16 + g4 * 4) = m;
    }
    BAR();
    {
      const LAS f32x4* mx = reinterpret_cast<const LAS f32x4*>(smc + S_MX);
      const f32x4 m0 = mx[0], m1 = mx[1], m2 = mx[2], m3 = mx[3];
      float pr[4];
#pragma unroll
      for (int h = 0; h < 4; ++h) {
        const float M = fmaxf(fmaxf(m0[h], m1[h]), fmaxf(m2[h], m3[h]));
        pr[h] = __builtin_amdgcn_exp2f((sc[h] - M) * LOG2E);
      }
      const unsigned u0 = cvtpk(pr[0], pr[1]), u1 = cvtpk(pr[2], pr[3]);
      LAS unsigned short* pp = reinterpret_cast<LAS unsigned short*>(sm + S_P) + 64 * wave + lane;
      pp[0] = (unsigned short)u0; pp[P_ST / 2] = (unsigned short)(u0 >> 16); pp[P_ST] = (unsigned short)u1; pp[3 * P_ST / 2] = (unsigned short)(u1 >> 16);
      const float l = row16_sum(tr16(tr32(bflo(u0), bflo(u1)), tr32(bfhi(u0), bfhi(u1))));
      if ((lane & 15) == 0) *reinterpret_cast<LAS float*>(sm + S_LS + wave * 16 + g4 * 4) = l;
    }
    {
      if (!__all(TAGOK(gg))) {
#pragma nounroll
        for (unsigned spins = 0; spins < SPIN_CAP; ++spins) {
          MEM_FENCE();
          if (!TAGOK(gg)) gg = ld_sc1(rs, goff);
          if (__all(TAGOK(gg))) break;
        }
      }
      float s0 = __uint_as_float(gg.y), s1 = __uint_as_float(gg.w);
      s0 += dpp<XOR1>(s0); s0 += dpp<XOR2>(s0); s1 += dpp<XOR1>(s1); s1 += dpp<XOR2>(s1);
      const unsigned own = cvtpk(s0, s1), oth = cvtpk(dpp<XOR7>(s0), dpp<XOR7>(s1));
      const float g0 = bflo(own), g1 = bfhi(own);
      const unsigned sg = cvtpk(g0 * __builtin_amdgcn_rcpf(1.f + __expf(-g0)), g1 * __builtin_amdgcn_rcpf(1.f + __expf(-g1)));
      const unsigned hh = cvtpk(bflo(sg) * bflo(oth), bfhi(sg) * bfhi(oth));
      if (gact && (lane & 7) == 0) *reinterpret_cast<LAS unsigned*>(sm + S_H + gb * H_ST + gcp * 4) = hh;
    }
    BAR();
    if (wave == 3 && lane < 8) {
      const int h = lane & 3;
      const LAS float* mx = reinterpret_cast<const LAS float*>(smc + S_MX);
      const LAS float* ls = reinterpret_cast<const LAS float*>(smc + S_LS);
      const float M = fmaxf(fmaxf(mx[h], mx[4 + h]), fmaxf(mx[8 + h], mx[12 + h]));
      const float L = (ls[h] + ls[4 + h]) + (ls[8 + h] + ls[12 + h]);
      st_sc1(rs, WS_ML + (((ab * HKV + kvh) * NSPLIT + asplit) * 8 + lane) * 16, v4u{tA, __float_as_uint(M), tB, __float_as_uint(L)});
    }
    {
      f32x4 a0 = {0.f, 0.f, 0.f, 0.f}, a1 = {0.f, 0.f, 0.f, 0.f};
      const LAS char* pa = smc + S_P + (lane & 3) * P_ST + g4 * 16;
#pragma unroll
      for (int ks = 0; ks < 8; ++ks) {
        if (ks == 6) BAR();
        const v4u a = lds128(pa + ks * 64);
        const int r0 = 32 * ks + 8 * g4 + q4, c0 = 4 * wave + (p4 >> 1), e = 8 * (p4 & 1);
        a0 = mfma(a, ldtr(smc + LV + off256(r0, c0) + e, smc + LV + off256(r0 + 4, c0) + e), a0);
        a1 = mfma(a, ldtr(smc + LV + off256(r0, c0 + 2) + e, smc + LV + off256(r0 + 4, c0 + 2) + e), a1);
      }
      if (lane < 16) {
        const int po = WS_PO + ((((ab * HKV + kvh) * 2) * NSPLIT + asplit) * 128 + 32 * wave + lane) * 16;
        st_sc1(rs, po, v4u{tA, __float_as_uint(a0[0]), tB, __float_as_uint(a0[1])});
        st_sc1(rs, po + 256, v4u{tA, __float_as_uint(a1[0]), tB, __float_as_uint(a1[1])});
        st_sc1(rs, po + NSPLIT * 128 * 16, v4u{tA, __float_as_uint(a0[2]), tB, __float_as_uint(a0[3])});
        st_sc1(rs, po + NSPLIT * 128 * 16 + 256, v4u{tA, __float_as_uint(a1[2]), tB, __float_as_uint(a1[3])});
      }
    }
    BAR();
    {
      const int cb = wave >> 1, cs = lane & 15, d0 = (wave & 1) * 16 + g4;
      const int ooff = WS_PO + ((((cb * HKV + kvh) * 2 + hp) * NSPLIT + cs) * 128 + 32 * dq + d0) * 16;
      const int moff = WS_ML + (((cb * HKV + kvh) * NSPLIT + cs) * 8 + 2 * hp) * 16;
      LAS unsigned short* od = reinterpret_cast<LAS unsigned short*>(sm + S_O) + cb * (O_ST / 2) + ((lane >> 1) & 1) * 32 + d0 + 4 * ((lane >> 2) & 3);
      BAR();
      __builtin_amdgcn_s_sleep(D3A);
      v4u go[4], gm0, gm1;
      gm0 = ld_sc1(rs, moff); gm1 = ld_sc1(rs, moff + 16);
#pragma unroll
      for (int j = 0; j < 4; ++j) go[j] = ld_sc1(rs, ooff + 64 * j);
      if (!__all(TAGOK(gm0) && TAGOK(gm1))) {
#pragma nounroll
        for (unsigned spins = 0; spins < SPIN_CAP; ++spins) {
          MEM_FENCE();
          if (!TAGOK(gm0)) gm0 = ld_sc1(rs, moff);
          if (!TAGOK(gm1)) gm1 = ld_sc1(rs, moff + 16);
          if (__all(TAGOK(gm0) && TAGOK(gm1))) break;
        }
      }
      const float ma = __uint_as_float(gm0.y), mb = __uint_as_float(gm1.y);
      const float wa = __builtin_amdgcn_exp2f((ma - row16_max(ma)) * LOG2E), wb = __builtin_amdgcn_exp2f((mb - row16_max(mb)) * LOG2E);
      const float na = wa * __builtin_amdgcn_rcpf(row16_sum(wa * __uint_as_float(gm0.w)));
      const float nb = wb * __builtin_amdgcn_rcpf(row16_sum(wb * __uint_as_float(gm1.w)));
      asm volatile("" :: "v"(na), "v"(nb));
#define ALLOK (TAGOK(go[0]) && TAGOK(go[1]) && TAGOK(go[2]) && TAGOK(go[3]))
      if (!__all(ALLOK)) {
#pragma nounroll
        for (unsigned spins = 0; spins < SPIN_CAP; ++spins) {
          __builtin_amdgcn_s_sleep(D3B);
          MEM_FENCE();
#pragma unroll
          for (int j = 0; j < 4; ++j)
            if (!TAGOK(go[j])) go[j] = ld_sc1(rs, ooff + 64 * j);
          if (__all(ALLOK)) break;
        }
      }
#undef ALLOK
      float v[4][2];
#pragma unroll
      for (int j = 0; j < 4; ++j) { v[j][0] = na * __uint_as_float(go[j].y); v[j][1] = nb * __uint_as_float(go[j].w); }
      float u[2][2], w2[2];
#pragma unroll
      for (int j = 0; j < 2; ++j)
#pragma unroll
        for (int h = 0; h < 2; ++h) { const float a = v[j][h] + dpp<XOR8>(v[j][h]), b = v[j + 2][h] + dpp<XOR8>(v[j + 2][h]); u[j][h] = (lane & 8) ? b : a; }
#pragma unroll
      for (int h = 0; h < 2; ++h) { const float a = u[0][h] + dpp<XOR7>(u[0][h]), b = u[1][h] + dpp<XOR7>(u[1][h]); w2[h] = (lane & 4) ? b : a; }
      const float a = w2[0] + dpp<XOR2>(w2[0]), b = w2[1] + dpp<XOR2>(w2[1]);
      float r = (lane & 2) ? b : a;
      r += dpp<XOR1>(r);
      if ((lane & 1) == 0) *od = (unsigned short)cvtpk(r, r);
    }
    BAR();
  }

  if (wave == 0) {
    const int ob = (xcd * 32 + idx) * 4;
    const unsigned short xfin = p.x[ob + g4];
    v4u g[4];
    const int o0 = WS_F + (lane * 1024 + ob) * 16;
    __builtin_amdgcn_s_sleep(D4A);
#pragma unroll
    for (int k = 0; k < 4; ++k) g[k] = ld_sc1(rs, o0 + k * 16);
    if (!__all(TAGOK(g[0]) && TAGOK(g[1]) && TAGOK(g[2]) && TAGOK(g[3]))) {
#pragma nounroll
      for (unsigned spins = 0; spins < SPIN_CAP; ++spins) {
        __builtin_amdgcn_s_sleep(D4B);
        MEM_FENCE();
#pragma unroll
        for (int k = 0; k < 4; ++k)
          if (!TAGOK(g[k])) g[k] = ld_sc1(rs, o0 + k * 16);
        if (__all(TAGOK(g[0]) && TAGOK(g[1]) && TAGOK(g[2]) && TAGOK(g[3]))) break;
      }
    }
    float va = tr16(tr32(__uint_as_float(g[0].y), __uint_as_float(g[2].y)), tr32(__uint_as_float(g[1].y), __uint_as_float(g[3].y)));
    float vb = tr16(tr32(__uint_as_float(g[0].w), __uint_as_float(g[2].w)), tr32(__uint_as_float(g[1].w), __uint_as_float(g[3].w)));
    va = row16_sum(va); vb = row16_sum(vb);
    if ((lane & 15) == 0) {
      const float r = bfr(bflo(xfin) + bfr(va)) + bfr(vb);
      p.out[ob + g4] = (unsigned short)cvtpk(r, r);
      if (bid == 0 && lane == 0) *reinterpret_cast<volatile u64*>(p.ws) = epoch + 1ull;
    }
  }
}

extern "C" void kernel_launch(void* const* d_in, const int* in_sizes, int n_in, void* d_out, int out_size, void* d_ws,
                              size_t ws_size, hipStream_t stream) {
  Params p;
  p.x = (const unsigned short*)d_in[0];
  p.kc = (const unsigned short*)d_in[1];
  p.vc = (const unsigned short*)d_in[2];
  p.wq = (const unsigned short*)d_in[3];
  p.wo = (const unsigned short*)d_in[4];
  p.wg = (const unsigned short*)d_in[6];
  p.wu = (const unsigned short*)d_in[7];
  p.wd = (const unsigned short*)d_in[8];
  p.out = (unsigned short*)d_out;
  p.ws = (unsigned char*)d_ws;
  (void)hipFuncSetAttribute((const void*)fused_decode_kernel, hipFuncAttributeMaxDynamicSharedMemorySize, LDS_BYTES);
  hipLaunchKernelGGL(fused_decode_kernel, dim3(NWG), dim3(NTHR), LDS_BYTES, stream, p);
}
```
